```python
import torch

def _softmax(x, axis=-1):
    return torch.softmax(x, dim=axis)
def _gelu(x):
    return torch.nn.functional.gelu(x, approximate="tanh")
def _silu(x):
    return torch.nn.functional.silu(x)
def _sigmoid(x):
    return torch.sigmoid(x)
def _layernorm(x, axis=-1, eps=1e-5):
    m = x.mean(dim=axis, keepdim=True)
    v = x.var(dim=axis, keepdim=True, unbiased=False)
    return (x - m) / torch.sqrt(v + eps)
def _scatter_add(tgt, idx, src, axis=0):
    return tgt.clone().index_add_(axis, idx, src)
def _segment_sum(src, seg, n):
    out = torch.zeros((n,) + src.shape[1:], dtype=src.dtype)
    return out.index_add_(0, seg, src)
def _pad(x, pw):
    flat = tuple(v for pair in reversed(pw) for v in pair)
    return torch.nn.functional.pad(x, flat)

kernel_name = 'standalone__batchnorm__sampling'

def setup_inputs():

    _w = (lambda a: (lambda t: t.to(torch.bfloat16) if t.is_floating_point() else t)(torch.as_tensor(a)))
    B, S, C = 1024, 16, 448
    K = 3
    return {
        "x": _w((lambda *s: torch.randn(*s, dtype=torch.bfloat16) * 0.05)(B, S, C)),
        "w": _w((lambda *s: torch.randn(*s, dtype=torch.bfloat16) * 0.05)(K, C, C) * (1.0 / (K * C) ** 0.5)),
        "mean": _w((lambda *s: torch.randn(*s, dtype=torch.bfloat16) * 0.05)(C) * 0.1),
        "var": _w(torch.abs((lambda *s: torch.randn(*s, dtype=torch.bfloat16) * 0.05)(C)) + 0.5),
        "g": _w((lambda *s: torch.randn(*s, dtype=torch.bfloat16) * 0.05)(C) + 1.0),
        "b": _w((lambda *s: torch.randn(*s, dtype=torch.bfloat16) * 0.05)(C) * 0.1),
    }

def reference(x, w, mean, var, g, b):
    K = 3
    P = K // 2
    S = x.shape[1]
    xp = _pad(x, ((0, 0), (P, P), (0, 0)))
    y = sum(torch.einsum('bsc,cd->bsd', xp[:, k:k + S, :], w[k]) for k in range(K))
    inv_std = 1.0 / torch.sqrt(var + 1e-5)
    y = (y - mean) * (g * inv_std) + b
    return _silu(y)
```

```cpp
#include <hip/hip_runtime.h>
#include <hip/hip_bf16.h>
#include <stdint.h>

typedef float f32x4 __attribute__((ext_vector_type(4)));
typedef __bf16 bf16x8 __attribute__((ext_vector_type(8)));
typedef unsigned short u16;

static constexpr int B_ = 1024, S_ = 16, C_ = 448;

__device__ inline float bf2f(u16 v) { return __uint_as_float(((unsigned)v) << 16); }

__global__ __launch_bounds__(256) void k_naive(const u16* __restrict__ x, const u16* __restrict__ w,
    const u16* __restrict__ mean, const u16* __restrict__ var, const u16* __restrict__ g, const u16* __restrict__ bb,
    u16* __restrict__ out) {
  int wave = (blockIdx.x * 256 + threadIdx.x) >> 6;
  int lane = threadIdx.x & 63;
  int b = wave / 28, dt = wave % 28;
  int r = lane & 15, q = lane >> 4;
  f32x4 acc = {0, 0, 0, 0};
  for (int k = 0; k < 3; ++k) {
    int s = r + k - 1;
    bool ok = (s >= 0 && s < S_);
    for (int c0 = 0; c0 < C_; c0 += 32) {
      bf16x8 xa, wa;
      u16 tx[8], tw[8];
      for (int j = 0; j < 8; ++j) {
        int c = c0 + 8 * q + j;
        tx[j] = ok ? x[((size_t)b * S_ + s) * C_ + c] : (u16)0;
        tw[j] = w[((size_t)k * C_ + c) * C_ + dt * 16 + r];
      }
      for (int j = 0; j < 8; ++j) { xa[j] = __builtin_bit_cast(__bf16, tx[j]); wa[j] = __builtin_bit_cast(__bf16, tw[j]); }
      acc = __builtin_amdgcn_mfma_f32_16x16x32_bf16(wa, xa, acc, 0, 0, 0);
    }
  }
  for (int j = 0; j < 4; ++j) {
    int d = dt * 16 + 4 * q + j;
    float inv = 1.0f / sqrtf(bf2f(var[d]) + 1e-5f);
    float sc = bf2f(g[d]) * inv;
    float t = (acc[j] - bf2f(mean[d])) * sc + bf2f(bb[d]);
    float o = t / (1.0f + __expf(-t));
    __bf16 ob = (__bf16)o;
    out[((size_t)b * S_ + r) * C_ + d] = __builtin_bit_cast(u16, ob);
  }
}

extern "C" void kernel_launch(void* const* d_in, const int* in_sizes, int n_in,
                              void* d_out, int out_size, void* d_ws, size_t ws_size,
                              hipStream_t stream) {
  const u16* x = (const u16*)d_in[0];
  const u16* w = (const u16*)d_in[1];
  const u16* mean = (const u16*)d_in[2];
  const u16* var = (const u16*)d_in[3];
  const u16* g = (const u16*)d_in[4];
  const u16* b = (const u16*)d_in[5];
  u16* out = (u16*)d_out;
  k_naive<<<B_ * 28 / 4, 256, 0, stream>>>(x, w, mean, var, g, b, out);
}
```

```cpp
#include <hip/hip_runtime.h>
#include <stdint.h>

__global__ __launch_bounds__(256, 1) void conv_bn_silu_kernel(const void* __restrict__ x, const void* __restrict__ w, const void* __restrict__ mean,
    const void* __restrict__ var, const void* __restrict__ g, const void* __restrict__ b, void* __restrict__ out) {
  const unsigned wave = __builtin_amdgcn_readfirstlane(threadIdx.x >> 6);
  const unsigned blk = blockIdx.x;
  const unsigned long long kp = (unsigned long long)(uintptr_t)__builtin_amdgcn_kernarg_segment_ptr();
  asm volatile(
    "s_load_dwordx8 s[68:75], %[kp], 0x0\n"
    "s_load_dwordx4 s[76:79], %[kp], 0x20\n"
    "s_load_dwordx2 s[80:81], %[kp], 0x30\n"
    "s_getpc_b64 s[62:63]\n"
    "8:\n"
    "s_mov_b32 s61, %[blk]\n"
    "s_mov_b32 s60, %[wave]\n"
    "v_mbcnt_lo_u32_b32 v255, -1, 0\n"
    "v_mbcnt_hi_u32_b32 v255, -1, v255\n"
    "s_lshr_b32 s56, s60, 1\n"
    "s_and_b32 s57, s60, 1\n"
    "s_and_b32 s52, s61, 7\n"
    "s_lshr_b32 s53, s61, 3\n"
    "s_lshl_b32 s59, s53, 2\n"
    "s_add_u32 s59, s59, s60\n"
    "s_cmp_lt_u32 s53, 4\n"
    "s_cbranch_scc1 3f\n"
    "s_sub_u32 s53, s53, 4\n"
    "s_mul_i32 s54, s53, 37\n"
    "s_lshr_b32 s54, s54, 8\n"
    "s_mul_i32 s55, s54, 7\n"
    "s_sub_u32 s58, s53, s55\n"
    "s_lshl_b32 s52, s52, 3\n"
    "s_lshl_b32 s54, s54, 1\n"
    "s_add_u32 s52, s52, s54\n"
    "s_add_u32 s52, s52, s56\n"
    "s_mul_i32 s48, s52, 229376\n"
    "s_add_u32 s38, s48, 0\n"
    "s_mul_i32 s52, s56, 8192\n"
    "s_lshl_b32 s53, s57, 10\n"
    "s_add_u32 s46, s52, s53\n"
    "v_lshrrev_b32 v244, 3, v255\n"
    "v_and_b32 v245, 7, v255\n"
    "s_lshl_b32 s52, s57, 3\n"
    "v_add_u32 v246, s52, v244\n"
    "v_lshrrev_b32 v247, 1, v246\n"
    "v_xor_b32 v247, v247, v245\n"
    "v_lshlrev_b32 v247, 4, v247\n"
    "v_mul_u32_u24 v216, 14336, v246\n"
    "v_add_u32 v216, v216, v247\n"
    "v_and_b32 v247, 2, v244\n"
    "s_lshl_b32 s52, s57, 2\n"
    "v_or_b32 v247, s52, v247\n"
    "v_xor_b32 v247, v247, v245\n"
    "v_lshlrev_b32 v247, 4, v247\n"
    "s_lshl_b32 s52, s60, 3\n"
    "v_add_u32 v246, s52, v244\n"
    "v_mul_u32_u24 v246, 896, v246\n"
    "v_add_u32 v246, v246, v247\n"
    "v_add_u32 v219, 401408, v246\n"
    "v_add_u32 v220, 430080, v246\n"
    "v_add_u32 v217, 0, v246\n"
    "v_add_u32 v218, 28672, v246\n"
    "v_add_u32 v221, 802816, v246\n"
    "v_add_u32 v222, 831488, v246\n"
    "s_lshl_b32 s36, s58, 7\n"
    "s_lshl_b32 s45, s60, 10\n"
    "s_add_u32 s39, s48, 896\n"
    "s_add_u32 s40, s48, 1792\n"
    "s_add_u32 s41, s48, 2688\n"
    "s_mov_b32 s10, 14680064\n"
    "s_mov_b32 s11, 0x20000\n"
    "s_mov_b32 s14, 1204224\n"
    "s_mov_b32 s15, 0x20000\n"
    "s_waitcnt lgkmcnt(0)\n"
    "s_mov_b32 s8, s68\n"
    "s_and_b32 s9, s69, 0xffff\n"
    "s_add_u32 m0, s46, 135168\n"
    "s_nop 0\n"
    "buffer_load_dwordx4 v216, s[8:11], s38 offen offset:0 lds\n"
    "s_mov_b32 s12, s70\n"
    "s_and_b32 s13, s71, 0xffff\n"
    "s_add_u32 s37, s36, 0\n"
    "s_add_u32 m0, s45, 118784\n"
    "s_nop 0\n"
    "buffer_load_dwordx4 v219, s[12:15], s37 offen lds\n"
    "s_add_u32 m0, s45, 110592\n"
    "s_mov_b32 s20, s62\n"
    "s_and_b32 s21, s63, 0xffff\n"
    "s_mov_b32 s22, 5f-8b\n"
    "s_mov_b32 s23, 0x20000\n"
    "s_sub_u32 s52, s59, 16\n"
    "buffer_load_dwordx4 v217, s[12:15], s37 offen lds\n"
    "s_add_u32 m0, s45, 126976\n"
    "s_mul_i32 s52, s52, 2\n"
    "v_add_u32 v250, s52, v255\n"
    "v_lshlrev_b32 v250, 7, v250\n"
    "s_lshl_b32 s52, s61, 2\n"
    "s_add_u32 s52, s52, s60\n"
    "buffer_load_dwordx4 v221, s[12:15], s37 offen lds\n"
    "s_add_u32 m0, s46, 137216\n"
    "s_mul_i32 s52, s52, 8\n"
    "v_add_u32 v210, s52, v255\n"
    "v_mul_u32_u24 v211, 6242, v210\n"
    "v_lshrrev_b32 v211, 24, v211\n"
    "v_add_u32 v211, 1, v211\n"
    "buffer_load_dwordx4 v216, s[8:11], s39 offen offset:0 lds\n"
    "s_add_u32 m0, s46, 139264\n"
    "v_mul_u32_u24 v211, 448, v211\n"
    "v_add_u32 v210, v210, v211\n"
    "v_lshlrev_b32 v210, 7, v210\n"
    "s_lshl_b32 s53, s58, 2\n"
    "s_add_u32 s53, s53, s60\n"
    "buffer_load_dwordx4 v216, s[8:11], s40 offen offset:0 lds\n"
    "s_add_u32 m0, s46, 141312\n"
    "s_mul_i32 s54, s56, 229376\n"
    "s_sub_u32 s54, s48, s54\n"
    "v_and_b32 v211, 31, v255\n"
    "v_mul_u32_u24 v211, 14336, v211\n"
    "v_add_u32 v211, s54, v211\n"
    "buffer_load_dwordx4 v216, s[8:11], s41 offen offset:0 lds\n"
    "s_add_u32 m0, s45, 122880\n"
    "s_mul_i32 s54, s53, 43\n"
    "s_lshr_b32 s54, s54, 8\n"
    "s_mul_i32 s55, s54, 6\n"
    "s_sub_u32 s55, s53, s55\n"
    "s_lshl_b32 s55, s55, 7\n"
    "buffer_load_dwordx4 v220, s[12:15], s37 offen lds\n"
    "s_add_u32 m0, s45, 114688\n"
    "s_mul_i32 s54, s54, 896\n"
    "s_add_u32 s54, s54, s55\n"
    "s_add_u32 s54, s54, 128\n"
    "s_cmp_lt_u32 s53, 24\n"
    "s_cselect_b32 s54, s54, 2132803568\n"
    "buffer_load_dwordx4 v218, s[12:15], s37 offen lds\n"
    "s_add_u32 m0, s45, 131072\n"
    "v_add_u32 v211, s54, v211\n"
    "buffer_load_dwordx4 v222, s[12:15], s37 offen lds\n"
    "s_mov_b64 exec, 0x3\n"
    "buffer_load_dword v253, v250, s[20:23], 0 offen\n"
    "s_mov_b64 exec, -1\n"
    "s_mov_b64 exec, 0xff\n"
    "buffer_load_dword v253, v210, s[12:15], 0 offen\n"
    "s_mov_b64 exec, -1\n"
    "s_mov_b32 exec_hi, 0\n"
    "buffer_load_dword v253, v211, s[8:11], 0 offen\n"
    "s_mov_b64 exec, -1\n"
    "s_add_u32 m0, s46, 53120\n"
    "v_lshrrev_b32 v244, 4, v255\n"
    "v_bfe_u32 v245, v255, 2, 2\n"
    "v_and_b32 v246, 3, v255\n"
    "v_and_b32 v248, 2, v245\n"
    "v_and_b32 v249, 1, v244\n"
    "v_lshl_or_b32 v248, v249, 2, v248\n"
    "s_lshl_b32 s52, s57, 2\n"
    "v_or_b32 v249, s52, v246\n"
    "v_xor_b32 v249, v249, v248\n"
    "v_lshlrev_b32 v249, 4, v249\n"
    "v_lshl_add_u32 v249, v244, 10, v249\n"
    "buffer_load_dwordx4 v216, s[8:11], s38 offen offset:128 lds\n"
    "s_add_u32 s37, s36, 57344\n"
    "s_add_u32 m0, s45, 36864\n"
    "v_lshl_add_u32 v249, v245, 7, v249\n"
    "v_add_u32 v223, 0, v249\n"
    "v_add_u32 v224, 65536, v249\n"
    "v_add_u32 v225, 131072, v249\n"
    "v_add_u32 v226, 8, v249\n"
    "v_add_u32 v227, 65544, v249\n"
    "v_add_u32 v228, 131080, v249\n"
    "v_and_b32 v247, 15, v255\n"
    "v_lshrrev_b32 v248, 1, v247\n"
    "v_xor_b32 v248, v248, v244\n"
    "v_lshlrev_b32 v248, 4, v248\n"
    "buffer_load_dwordx4 v219, s[12:15], s37 offen lds\n"
    "s_add_u32 m0, s45, 28672\n"
    "v_and_b32 v249, 7, v247\n"
    "v_lshl_add_u32 v248, v249, 7, v248\n"
    "v_lshrrev_b32 v249, 3, v247\n"
    "v_lshl_add_u32 v248, v249, 10, v248\n"
    "v_xor_b32 v249, 64, v248\n"
    "s_mul_i32 s52, s56, 8192\n"
    "s_mul_i32 s53, s56, 14336\n"
    "s_lshl_b32 s55, s57, 10\n"
    "s_add_u32 s47, s53, s55\n"
    "s_add_u32 s54, s52, 0\n"
    "v_add_u32 v229, s54, v248\n"
    "buffer_load_dwordx4 v217, s[12:15], s37 offen lds\n"
    "s_add_u32 m0, s45, 45056\n"
    "s_add_u32 s54, s52, 65536\n"
    "v_add_u32 v230, s54, v248\n"
    "s_add_u32 s54, s52, 131072\n"
    "v_add_u32 v231, s54, v248\n"
    "s_add_u32 s54, s53, 0\n"
    "v_add_u32 v235, s54, v248\n"
    "s_add_u32 s54, s53, 65536\n"
    "v_add_u32 v236, s54, v248\n"
    "s_add_u32 s54, s52, 0\n"
    "v_add_u32 v232, s54, v249\n"
    "s_add_u32 s54, s52, 65536\n"
    "buffer_load_dwordx4 v221, s[12:15], s37 offen lds\n"
    "s_add_u32 m0, s46, 55168\n"
    "v_add_u32 v233, s54, v249\n"
    "s_add_u32 s54, s52, 131072\n"
    "v_add_u32 v234, s54, v249\n"
    "s_add_u32 s54, s53, 0\n"
    "v_add_u32 v238, s54, v249\n"
    "s_add_u32 s54, s53, 65536\n"
    "v_add_u32 v239, s54, v249\n"
    "s_lshl_b32 s52, s57, 2\n"
    "v_or_b32 v248, s52, v244\n"
    "v_and_b32 v249, 7, v247\n"
    "v_xor_b32 v248, v248, v249\n"
    "buffer_load_dwordx4 v216, s[8:11], s39 offen offset:128 lds\n"
    "s_add_u32 m0, s46, 57216\n"
    "v_lshlrev_b32 v248, 4, v248\n"
    "v_lshl_add_u32 v248, v247, 7, v248\n"
    "s_lshl_b32 s52, s56, 11\n"
    "s_add_u32 s52, s52, 147456\n"
    "v_add_u32 v241, s52, v248\n"
    "s_lshl_b32 s53, s57, 10\n"
    "s_add_u32 s52, s52, s53\n"
    "v_lshlrev_b32 v248, 4, v255\n"
    "v_add_u32 v242, s52, v248\n"
    "v_lshrrev_b32 v248, 3, v255\n"
    "v_and_b32 v250, 7, v255\n"
    "buffer_load_dwordx4 v216, s[8:11], s40 offen offset:128 lds\n"
    "s_add_u32 m0, s46, 59264\n"
    "v_xor_b32 v249, v248, v250\n"
    "v_lshlrev_b32 v249, 4, v249\n"
    "s_lshl_b32 s52, s57, 3\n"
    "v_add_u32 v248, s52, v248\n"
    "v_mul_u32_u24 v248, 14336, v248\n"
    "v_add_u32 v243, v248, v249\n"
    "s_lshl_b32 s52, s57, 6\n"
    "v_lshlrev_b32 v248, 4, v244\n"
    "v_add_u32 v254, s52, v248\n"
    "v_mul_u32_u24 v248, 14336, v247\n"
    "v_add_u32 v252, v248, v254\n"
    "buffer_load_dwordx4 v216, s[8:11], s41 offen offset:128 lds\n"
    "s_add_u32 m0, s45, 40960\n"
    "s_mov_b32 s16, s80\n"
    "s_and_b32 s17, s81, 0xffff\n"
    "s_mov_b32 s18, 14680064\n"
    "s_mov_b32 s19, 0x20000\n"
    "s_add_u32 s49, s48, s36\n"
    "s_mov_b32 s51, 0xbfb8aa3b\n"
    "s_mov_b32 s20, s72\n"
    "s_and_b32 s21, s73, 0xffff\n"
    "s_mov_b32 s22, 896\n"
    "s_mov_b32 s23, 0x20000\n"
    "s_mov_b32 s24, s74\n"
    "buffer_load_dwordx4 v220, s[12:15], s37 offen lds\n"
    "s_add_u32 m0, s45, 32768\n"
    "s_and_b32 s25, s75, 0xffff\n"
    "s_mov_b32 s26, 896\n"
    "s_mov_b32 s27, 0x20000\n"
    "s_mov_b32 s28, s76\n"
    "s_and_b32 s29, s77, 0xffff\n"
    "s_mov_b32 s30, 896\n"
    "s_mov_b32 s31, 0x20000\n"
    "s_mov_b32 s64, s78\n"
    "s_and_b32 s65, s79, 0xffff\n"
    "s_mov_b32 s66, 896\n"
    "s_mov_b32 s67, 0x20000\n"
    "buffer_load_dwordx4 v218, s[12:15], s37 offen lds\n"
    "s_add_u32 m0, s45, 49152\n"
    "s_nop 0\n"
    "buffer_load_dwordx4 v222, s[12:15], s37 offen lds\n"
    "s_add_u32 m0, s46, 93952\n"
    "s_nop 0\n"
    "buffer_load_dwordx4 v216, s[8:11], s38 offen offset:256 lds\n"
    "s_add_u32 s37, s36, 114688\n"
    "s_add_u32 m0, s45, 77824\n"
    "s_nop 0\n"
    "buffer_load_dwordx4 v219, s[12:15], s37 offen lds\n"
    "s_add_u32 m0, s45, 69632\n"
    "s_nop 0\n"
    "buffer_load_dwordx4 v217, s[12:15], s37 offen lds\n"
    "s_add_u32 m0, s45, 86016\n"
    "s_nop 0\n"
    "buffer_load_dwordx4 v221, s[12:15], s37 offen lds\n"
    "buffer_load_dwordx4 v[192:195], v254, s[20:23], s36 offen\n"
    "buffer_load_dwordx4 v[196:199], v254, s[24:27], s36 offen\n"
    "buffer_load_dwordx4 v[200:203], v254, s[28:31], s36 offen\n"
    "buffer_load_dwordx4 v[204:207], v254, s[64:67], s36 offen\n"
    "s_waitcnt vmcnt(21)\n"
    "s_barrier\n"
    "ds_read_b128 v[144:147], v231 offset:4096\n"
    "ds_read_b64_tr_b16 a[8:9], v224 offset:53248\n"
    "ds_read_b64_tr_b16 a[10:11], v224 offset:53760\n"
    "ds_read_b128 v[148:151], v231 offset:6144\n"
    "ds_read_b64_tr_b16 a[16:17], v224 offset:61440\n"
    "ds_read_b64_tr_b16 a[18:19], v224 offset:61952\n"
    "ds_read_b64_tr_b16 a[12:13], v227 offset:53248\n"
    "ds_read_b64_tr_b16 a[14:15], v227 offset:53760\n"
    "ds_read_b64_tr_b16 a[20:21], v227 offset:61440\n"
    "ds_read_b64_tr_b16 a[22:23], v227 offset:61952\n"
    "ds_read_b64_tr_b16 a[0:1], v224 offset:45056\n"
    "ds_read_b64_tr_b16 a[2:3], v224 offset:45568\n"
    "ds_read_b128 v[152:155], v231 offset:8192\n"
    "ds_read_b64_tr_b16 a[4:5], v227 offset:45056\n"
    "ds_read_b64_tr_b16 a[6:7], v227 offset:45568\n"
    "ds_read_b128 v[156:159], v231 offset:10240\n"
    "s_waitcnt lgkmcnt(13)\n"
    "v_mfma_f32_16x16x32_bf16 v[80:83], a[8:11], v[144:147], 0\n"
    "ds_read_b128 v[160:163], v234 offset:4096\n"
    "s_waitcnt lgkmcnt(11)\n"
    "v_mfma_f32_16x16x32_bf16 v[80:83], a[16:19], v[148:151], v[80:83]\n"
    "ds_read_b64_tr_b16 a[32:33], v224 offset:57344\n"
    "s_waitcnt lgkmcnt(10)\n"
    "v_mfma_f32_16x16x32_bf16 v[84:87], a[12:15], v[144:147], 0\n"
    "ds_read_b64_tr_b16 a[34:35], v224 offset:57856\n"
    "s_waitcnt lgkmcnt(9)\n"
    "v_mfma_f32_16x16x32_bf16 v[84:87], a[20:23], v[148:151], v[84:87]\n"
    "ds_read_b128 v[164:167], v234 offset:6144\n"
    "s_waitcnt lgkmcnt(8)\n"
    "v_mfma_f32_16x16x32_bf16 v[88:91], a[0:3], v[144:147], 0\n"
    "ds_read_b64_tr_b16 a[40:41], v225 offset:0\n"
    "s_add_u32 m0, s46, 96000\n"
    "v_mfma_f32_16x16x32_bf16 v[88:91], a[8:11], v[148:151], v[88:91]\n"
    "buffer_load_dwordx4 v216, s[8:11], s39 offen offset:256 lds\n"
    "s_waitcnt lgkmcnt(5)\n"
    "v_mfma_f32_16x16x32_bf16 v[88:91], a[16:19], v[152:155], v[88:91]\n"
    "ds_read_b64_tr_b16 a[42:43], v225 offset:512\n"
    "s_add_u32 m0, s46, 98048\n"
    "v_mfma_f32_16x16x32_bf16 v[92:95], a[4:7], v[144:147], 0\n"
    "ds_read_b64_tr_b16 a[36:37], v227 offset:57344\n"
    "v_mfma_f32_16x16x32_bf16 v[92:95], a[12:15], v[148:151], v[92:95]\n"
    "ds_read_b64_tr_b16 a[38:39], v227 offset:57856\n"
    "v_mfma_f32_16x16x32_bf16 v[92:95], a[20:23], v[152:155], v[92:95]\n"
    "buffer_load_dwordx4 v216, s[8:11], s40 offen offset:256 lds\n"
    "v_mfma_f32_16x16x32_bf16 v[96:99], a[0:3], v[148:151], 0\n"
    "ds_read_b64_tr_b16 a[44:45], v228 offset:0\n"
    "s_add_u32 m0, s46, 100096\n"
    "v_mfma_f32_16x16x32_bf16 v[96:99], a[8:11], v[152:155], v[96:99]\n"
    "ds_read_b64_tr_b16 a[46:47], v228 offset:512\n"
    "v_mfma_f32_16x16x32_bf16 v[96:99], a[16:19], v[156:159], v[96:99]\n"
    "ds_read_b64_tr_b16 a[24:25], v224 offset:49152\n"
    "v_mfma_f32_16x16x32_bf16 v[100:103], a[4:7], v[148:151], 0\n"
    "buffer_load_dwordx4 v216, s[8:11], s41 offen offset:256 lds\n"
    "v_mfma_f32_16x16x32_bf16 v[100:103], a[12:15], v[152:155], v[100:103]\n"
    "ds_read_b64_tr_b16 a[26:27], v224 offset:49664\n"
    "s_add_u32 m0, s45, 81920\n"
    "v_mfma_f32_16x16x32_bf16 v[100:103], a[20:23], v[156:159], v[100:103]\n"
    "ds_read_b128 v[168:171], v234 offset:8192\n"
    "v_mfma_f32_16x16x32_bf16 v[104:107], a[0:3], v[152:155], 0\n"
    "ds_read_b64_tr_b16 a[28:29], v227 offset:49152\n"
    "v_mfma_f32_16x16x32_bf16 v[104:107], a[8:11], v[156:159], v[104:107]\n"
    "buffer_load_dwordx4 v220, s[12:15], s37 offen lds\n"
    "v_mfma_f32_16x16x32_bf16 v[108:111], a[4:7], v[152:155], 0\n"
    "ds_read_b64_tr_b16 a[30:31], v227 offset:49664\n"
    "s_add_u32 m0, s45, 73728\n"
    "v_mfma_f32_16x16x32_bf16 v[108:111], a[12:15], v[156:159], v[108:111]\n"
    "ds_read_b128 v[172:175], v234 offset:10240\n"
    "v_mfma_f32_16x16x32_bf16 v[112:115], a[0:3], v[156:159], 0\n"
    "v_mfma_f32_16x16x32_bf16 v[116:119], a[4:7], v[156:159], 0\n"
    "buffer_load_dwordx4 v218, s[12:15], s37 offen lds\n"
    "s_waitcnt lgkmcnt(3)\n"
    "v_mfma_f32_16x16x32_bf16 v[80:83], a[32:35], v[160:163], v[80:83]\n"
    "s_waitcnt vmcnt(13) lgkmcnt(0)\n"
    "s_barrier\n"
    "v_mfma_f32_16x16x32_bf16 v[80:83], a[40:43], v[164:167], v[80:83]\n"
    "ds_read_b128 v[144:147], v229 offset:53248\n"
    "s_add_u32 m0, s45, 90112\n"
    "v_mfma_f32_16x16x32_bf16 v[84:87], a[36:39], v[160:163], v[84:87]\n"
    "ds_read_b64_tr_b16 a[56:57], v223 offset:36864\n"
    "v_mfma_f32_16x16x32_bf16 v[84:87], a[44:47], v[164:167], v[84:87]\n"
    "buffer_load_dwordx4 v222, s[12:15], s37 offen lds\n"
    "v_mfma_f32_16x16x32_bf16 v[88:91], a[24:27], v[160:163], v[88:91]\n"
    "ds_read_b64_tr_b16 a[58:59], v223 offset:37376\n"
    "s_add_u32 m0, s46, 134784\n"
    "v_mfma_f32_16x16x32_bf16 v[88:91], a[32:35], v[164:167], v[88:91]\n"
    "ds_read_b128 v[148:151], v229 offset:55296\n"
    "v_mfma_f32_16x16x32_bf16 v[88:91], a[40:43], v[168:171], v[88:91]\n"
    "ds_read_b64_tr_b16 a[64:65], v223 offset:45056\n"
    "v_mfma_f32_16x16x32_bf16 v[92:95], a[28:31], v[160:163], v[92:95]\n"
    "buffer_load_dwordx4 v216, s[8:11], s38 offen offset:384 lds\n"
    "v_mfma_f32_16x16x32_bf16 v[92:95], a[36:39], v[164:167], v[92:95]\n"
    "ds_read_b64_tr_b16 a[66:67], v223 offset:45568\n"
    "s_add_u32 s37, s36, 172032\n"
    "v_mfma_f32_16x16x32_bf16 v[92:95], a[44:47], v[168:171], v[92:95]\n"
    "ds_read_b64_tr_b16 a[60:61], v226 offset:36864\n"
    "s_add_u32 m0, s45, 118784\n"
    "v_mfma_f32_16x16x32_bf16 v[96:99], a[24:27], v[164:167], v[96:99]\n"
    "ds_read_b64_tr_b16 a[62:63], v226 offset:37376\n"
    "v_mfma_f32_16x16x32_bf16 v[96:99], a[32:35], v[168:171], v[96:99]\n"
    "buffer_load_dwordx4 v219, s[12:15], s37 offen lds\n"
    "v_mfma_f32_16x16x32_bf16 v[96:99], a[40:43], v[172:175], v[96:99]\n"
    "ds_read_b64_tr_b16 a[68:69], v226 offset:45056\n"
    "s_add_u32 m0, s45, 110592\n"
    "v_mfma_f32_16x16x32_bf16 v[100:103], a[28:31], v[164:167], v[100:103]\n"
    "ds_read_b64_tr_b16 a[70:71], v226 offset:45568\n"
    "v_mfma_f32_16x16x32_bf16 v[100:103], a[36:39], v[168:171], v[100:103]\n"
    "ds_read_b64_tr_b16 a[48:49], v223 offset:28672\n"
    "v_mfma_f32_16x16x32_bf16 v[100:103], a[44:47], v[172:175], v[100:103]\n"
    "buffer_load_dwordx4 v217, s[12:15], s37 offen lds\n"
    "v_mfma_f32_16x16x32_bf16 v[104:107], a[24:27], v[168:171], v[104:107]\n"
    "ds_read_b64_tr_b16 a[50:51], v223 offset:29184\n"
    "s_add_u32 m0, s45, 126976\n"
    "v_mfma_f32_16x16x32_bf16 v[104:107], a[32:35], v[172:175], v[104:107]\n"
    "ds_read_b128 v[152:155], v229 offset:57344\n"
    "v_mfma_f32_16x16x32_bf16 v[108:111], a[28:31], v[168:171], v[108:111]\n"
    "ds_read_b64_tr_b16 a[52:53], v226 offset:28672\n"
    "v_mfma_f32_16x16x32_bf16 v[108:111], a[36:39], v[172:175], v[108:111]\n"
    "buffer_load_dwordx4 v221, s[12:15], s37 offen lds\n"
    "v_mfma_f32_16x16x32_bf16 v[112:115], a[24:27], v[172:175], v[112:115]\n"
    "ds_read_b64_tr_b16 a[54:55], v226 offset:29184\n"
    "s_add_u32 m0, s46, 136832\n"
    "v_mfma_f32_16x16x32_bf16 v[116:119], a[28:31], v[172:175], v[116:119]\n"
    "ds_read_b128 v[156:159], v229 offset:59392\n"
    "s_waitcnt lgkmcnt(5)\n"
    "v_mfma_f32_16x16x32_bf16 v[80:83], a[56:59], v[144:147], v[80:83]\n"
    "ds_read_b128 v[160:163], v232 offset:53248\n"
    "v_mfma_f32_16x16x32_bf16 v[80:83], a[64:67], v[148:151], v[80:83]\n"
    "buffer_load_dwordx4 v216, s[8:11], s39 offen offset:384 lds\n"
    "v_mfma_f32_16x16x32_bf16 v[84:87], a[60:63], v[144:147], v[84:87]\n"
    "ds_read_b64_tr_b16 a[80:81], v223 offset:40960\n"
    "s_add_u32 m0, s46, 138880\n"
    "v_mfma_f32_16x16x32_bf16 v[84:87], a[68:71], v[148:151], v[84:87]\n"
    "ds_read_b64_tr_b16 a[82:83], v223 offset:41472\n"
    "s_waitcnt lgkmcnt(5)\n"
    "v_mfma_f32_16x16x32_bf16 v[88:91], a[48:51], v[144:147], v[88:91]\n"
    "ds_read_b128 v[164:167], v232 offset:55296\n"
    "v_mfma_f32_16x16x32_bf16 v[88:91], a[56:59], v[148:151], v[88:91]\n"
    "buffer_load_dwordx4 v216, s[8:11], s40 offen offset:384 lds\n"
    "v_mfma_f32_16x16x32_bf16 v[88:91], a[64:67], v[152:155], v[88:91]\n"
    "ds_read_b64_tr_b16 a[88:89], v223 offset:49152\n"
    "s_waitcnt lgkmcnt(4)\n"
    "v_mfma_f32_16x16x32_bf16 v[92:95], a[52:55], v[144:147], v[92:95]\n"
    "ds_read_b64_tr_b16 a[90:91], v223 offset:49664\n"
    "s_add_u32 m0, s46, 140928\n"
    "v_mfma_f32_16x16x32_bf16 v[92:95], a[60:63], v[148:151], v[92:95]\n"
    "ds_read_b64_tr_b16 a[84:85], v226 offset:40960\n"
    "v_mfma_f32_16x16x32_bf16 v[92:95], a[68:71], v[152:155], v[92:95]\n"
    "buffer_load_dwordx4 v216, s[8:11], s41 offen offset:384 lds\n"
    "v_mfma_f32_16x16x32_bf16 v[96:99], a[48:51], v[148:151], v[96:99]\n"
    "ds_read_b64_tr_b16 a[86:87], v226 offset:41472\n"
    "s_add_u32 m0, s45, 122880\n"
    "v_mfma_f32_16x16x32_bf16 v[96:99], a[56:59], v[152:155], v[96:99]\n"
    "ds_read_b64_tr_b16 a[92:93], v226 offset:49152\n"
    "v_mfma_f32_16x16x32_bf16 v[96:99], a[64:67], v[156:159], v[96:99]\n"
    "ds_read_b64_tr_b16 a[94:95], v226 offset:49664\n"
    "v_mfma_f32_16x16x32_bf16 v[100:103], a[52:55], v[148:151], v[100:103]\n"
    "buffer_load_dwordx4 v220, s[12:15], s37 offen lds\n"
    "v_mfma_f32_16x16x32_bf16 v[100:103], a[60:63], v[152:155], v[100:103]\n"
    "ds_read_b64_tr_b16 a[72:73], v223 offset:32768\n"
    "s_add_u32 m0, s45, 114688\n"
    "v_mfma_f32_16x16x32_bf16 v[100:103], a[68:71], v[156:159], v[100:103]\n"
    "ds_read_b64_tr_b16 a[74:75], v223 offset:33280\n"
    "v_mfma_f32_16x16x32_bf16 v[104:107], a[48:51], v[152:155], v[104:107]\n"
    "ds_read_b128 v[168:171], v232 offset:57344\n"
    "v_mfma_f32_16x16x32_bf16 v[104:107], a[56:59], v[156:159], v[104:107]\n"
    "buffer_load_dwordx4 v218, s[12:15], s37 offen lds\n"
    "v_mfma_f32_16x16x32_bf16 v[108:111], a[52:55], v[152:155], v[108:111]\n"
    "ds_read_b64_tr_b16 a[76:77], v226 offset:32768\n"
    "s_add_u32 m0, s45, 131072\n"
    "v_mfma_f32_16x16x32_bf16 v[108:111], a[60:63], v[156:159], v[108:111]\n"
    "ds_read_b64_tr_b16 a[78:79], v226 offset:33280\n"
    "v_mfma_f32_16x16x32_bf16 v[112:115], a[48:51], v[156:159], v[112:115]\n"
    "ds_read_b128 v[172:175], v232 offset:59392\n"
    "v_mfma_f32_16x16x32_bf16 v[116:119], a[52:55], v[156:159], v[116:119]\n"
    "buffer_load_dwordx4 v222, s[12:15], s37 offen lds\n"
    "s_waitcnt lgkmcnt(4)\n"
    "v_mfma_f32_16x16x32_bf16 v[80:83], a[80:83], v[160:163], v[80:83]\n"
    "v_mfma_f32_16x16x32_bf16 v[80:83], a[88:91], v[164:167], v[80:83]\n"
    "s_waitcnt vmcnt(10) lgkmcnt(0)\n"
    "s_barrier\n"
    "v_mfma_f32_16x16x32_bf16 v[84:87], a[84:87], v[160:163], v[84:87]\n"
    "ds_read_b128 v[144:147], v230 offset:28672\n"
    "s_add_u32 m0, s46, 52736\n"
    "v_mfma_f32_16x16x32_bf16 v[84:87], a[92:95], v[164:167], v[84:87]\n"
    "buffer_load_dwordx4 v216, s[8:11], s38 offen offset:512 lds\n"
    "v_mfma_f32_16x16x32_bf16 v[88:91], a[72:75], v[160:163], v[88:91]\n"
    "ds_read_b64_tr_b16 a[104:105], v224 offset:12288\n"
    "s_add_u32 s37, s36, 229376\n"
    "v_mfma_f32_16x16x32_bf16 v[88:91], a[80:83], v[164:167], v[88:91]\n"
    "ds_read_b64_tr_b16 a[106:107], v224 offset:12800\n"
    "s_add_u32 m0, s45, 36864\n"
    "v_mfma_f32_16x16x32_bf16 v[88:91], a[88:91], v[168:171], v[88:91]\n"
    "ds_read_b128 v[148:151], v230 offset:30720\n"
    "v_lshlrev_b32_e32 v244, 16, v196\n"
    "v_mfma_f32_16x16x32_bf16 v[92:95], a[76:79], v[160:163], v[92:95]\n"
    "buffer_load_dwordx4 v219, s[12:15], s37 offen lds\n"
    "v_mfma_f32_16x16x32_bf16 v[92:95], a[84:87], v[164:167], v[92:95]\n"
    "ds_read_b64_tr_b16 a[112:113], v224 offset:20480\n"
    "s_add_u32 m0, s45, 28672\n"
    "v_mfma_f32_16x16x32_bf16 v[92:95], a[92:95], v[168:171], v[92:95]\n"
    "ds_read_b64_tr_b16 a[114:115], v224 offset:20992\n"
    "v_add_f32_e32 v244, 0x3727c5ac, v244\n"
    "v_mfma_f32_16x16x32_bf16 v[96:99], a[72:75], v[164:167], v[96:99]\n"
    "ds_read_b64_tr_b16 a[108:109], v227 offset:12288\n"
    "v_mfma_f32_16x16x32_bf16 v[96:99], a[80:83], v[168:171], v[96:99]\n"
    "buffer_load_dwordx4 v217, s[12:15], s37 offen lds\n"
    "v_mfma_f32_16x16x32_bf16 v[96:99], a[88:91], v[172:175], v[96:99]\n"
    "ds_read_b64_tr_b16 a[110:111], v227 offset:12800\n"
    "s_add_u32 m0, s45, 45056\n"
    "v_mfma_f32_16x16x32_bf16 v[100:103], a[76:79], v[164:167], v[100:103]\n"
    "ds_read_b64_tr_b16 a[116:117], v227 offset:20480\n"
    "v_mfma_f32_16x16x32_bf16 v[100:103], a[84:87], v[168:171], v[100:103]\n"
    "ds_read_b64_tr_b16 a[118:119], v227 offset:20992\n"
    "v_mfma_f32_16x16x32_bf16 v[100:103], a[92:95], v[172:175], v[100:103]\n"
    "buffer_load_dwordx4 v221, s[12:15], s37 offen lds\n"
    "v_mfma_f32_16x16x32_bf16 v[104:107], a[72:75], v[168:171], v[104:107]\n"
    "ds_read_b64_tr_b16 a[96:97], v224 offset:4096\n"
    "s_add_u32 m0, s46, 54784\n"
    "v_mfma_f32_16x16x32_bf16 v[104:107], a[80:83], v[172:175], v[104:107]\n"
    "ds_read_b64_tr_b16 a[98:99], v224 offset:4608\n"
    "v_mfma_f32_16x16x32_bf16 v[108:111], a[76:79], v[168:171], v[108:111]\n"
    "ds_read_b128 v[152:155], v230 offset:32768\n"
    "v_mfma_f32_16x16x32_bf16 v[108:111], a[84:87], v[172:175], v[108:111]\n"
    "buffer_load_dwordx4 v216, s[8:11], s39 offen offset:512 lds\n"
    "v_mfma_f32_16x16x32_bf16 v[112:115], a[72:75], v[172:175], v[112:115]\n"
    "ds_read_b64_tr_b16 a[100:101], v227 offset:4096\n"
    "s_add_u32 m0, s46, 56832\n"
    "v_mfma_f32_16x16x32_bf16 v[116:119], a[76:79], v[172:175], v[116:119]\n"
    "ds_read_b64_tr_b16 a[102:103], v227 offset:4608\n"
    "s_waitcnt lgkmcnt(5)\n"
    "v_mfma_f32_16x16x32_bf16 v[80:83], a[104:107], v[144:147], v[80:83]\n"
    "ds_read_b128 v[156:159], v230 offset:34816\n"
    "v_mfma_f32_16x16x32_bf16 v[80:83], a[112:115], v[148:151], v[80:83]\n"
    "buffer_load_dwordx4 v216, s[8:11], s40 offen offset:512 lds\n"
    "v_mfma_f32_16x16x32_bf16 v[84:87], a[108:111], v[144:147], v[84:87]\n"
    "ds_read_b128 v[160:163], v233 offset:28672\n"
    "s_add_u32 m0, s46, 58880\n"
    "v_mfma_f32_16x16x32_bf16 v[84:87], a[116:119], v[148:151], v[84:87]\n"
    "ds_read_b64_tr_b16 a[128:129], v224 offset:16384\n"
    "s_waitcnt lgkmcnt(5)\n"
    "v_mfma_f32_16x16x32_bf16 v[88:91], a[96:99], v[144:147], v[88:91]\n"
    "ds_read_b64_tr_b16 a[130:131], v224 offset:16896\n"
    "v_mfma_f32_16x16x32_bf16 v[88:91], a[104:107], v[148:151], v[88:91]\n"
    "buffer_load_dwordx4 v216, s[8:11], s41 offen offset:512 lds\n"
    "v_mfma_f32_16x16x32_bf16 v[88:91], a[112:115], v[152:155], v[88:91]\n"
    "ds_read_b128 v[164:167], v233 offset:30720\n"
    "s_waitcnt lgkmcnt(4)\n"
    "v_mfma_f32_16x16x32_bf16 v[92:95], a[100:103], v[144:147], v[92:95]\n"
    "ds_read_b64_tr_b16 a[136:137], v224 offset:24576\n"
    "s_add_u32 m0, s45, 40960\n"
    "v_mfma_f32_16x16x32_bf16 v[92:95], a[108:111], v[148:151], v[92:95]\n"
    "ds_read_b64_tr_b16 a[138:139], v224 offset:25088\n"
    "v_mfma_f32_16x16x32_bf16 v[92:95], a[116:119], v[152:155], v[92:95]\n"
    "buffer_load_dwordx4 v220, s[12:15], s37 offen lds\n"
    "v_mfma_f32_16x16x32_bf16 v[96:99], a[96:99], v[148:151], v[96:99]\n"
    "ds_read_b64_tr_b16 a[132:133], v227 offset:16384\n"
    "s_add_u32 m0, s45, 32768\n"
    "v_mfma_f32_16x16x32_bf16 v[96:99], a[104:107], v[152:155], v[96:99]\n"
    "ds_read_b64_tr_b16 a[134:135], v227 offset:16896\n"
    "v_mfma_f32_16x16x32_bf16 v[96:99], a[112:115], v[156:159], v[96:99]\n"
    "ds_read_b64_tr_b16 a[140:141], v227 offset:24576\n"
    "v_mfma_f32_16x16x32_bf16 v[100:103], a[100:103], v[148:151], v[100:103]\n"
    "buffer_load_dwordx4 v218, s[12:15], s37 offen lds\n"
    "v_mfma_f32_16x16x32_bf16 v[100:103], a[108:111], v[152:155], v[100:103]\n"
    "ds_read_b64_tr_b16 a[142:143], v227 offset:25088\n"
    "s_add_u32 m0, s45, 49152\n"
    "v_mfma_f32_16x16x32_bf16 v[100:103], a[116:119], v[156:159], v[100:103]\n"
    "ds_read_b64_tr_b16 a[120:121], v224 offset:8192\n"
    "v_mfma_f32_16x16x32_bf16 v[104:107], a[96:99], v[152:155], v[104:107]\n"
    "ds_read_b64_tr_b16 a[122:123], v224 offset:8704\n"
    "v_mfma_f32_16x16x32_bf16 v[104:107], a[104:107], v[156:159], v[104:107]\n"
    "buffer_load_dwordx4 v222, s[12:15], s37 offen lds\n"
    "v_mfma_f32_16x16x32_bf16 v[108:111], a[100:103], v[152:155], v[108:111]\n"
    "ds_read_b128 v[168:171], v233 offset:32768\n"
    "v_mfma_f32_16x16x32_bf16 v[108:111], a[108:111], v[156:159], v[108:111]\n"
    "ds_read_b64_tr_b16 a[124:125], v227 offset:8192\n"
    "v_mfma_f32_16x16x32_bf16 v[112:115], a[96:99], v[156:159], v[112:115]\n"
    "ds_read_b64_tr_b16 a[126:127], v227 offset:8704\n"
    "v_mfma_f32_16x16x32_bf16 v[116:119], a[100:103], v[156:159], v[116:119]\n"
    "ds_read_b128 v[172:175], v233 offset:34816\n"
    "s_waitcnt lgkmcnt(5)\n"
    "v_mfma_f32_16x16x32_bf16 v[80:83], a[128:131], v[160:163], v[80:83]\n"
    "v_rsq_f32_e32 v244, v244\n"
    "v_mfma_f32_16x16x32_bf16 v[80:83], a[136:139], v[164:167], v[80:83]\n"
    "v_lshlrev_b32_e32 v245, 16, v200\n"
    "v_mul_f32_e32 v245, s51, v245\n"
    "v_mfma_f32_16x16x32_bf16 v[84:87], a[132:135], v[160:163], v[84:87]\n"
    "s_waitcnt vmcnt(10) lgkmcnt(0)\n"
    "s_barrier\n"
    "v_mfma_f32_16x16x32_bf16 v[84:87], a[140:143], v[164:167], v[84:87]\n"
    "ds_read_b128 v[144:147], v231 offset:4096\n"
    "s_add_u32 m0, s46, 93568\n"
    "v_mfma_f32_16x16x32_bf16 v[88:91], a[120:123], v[160:163], v[88:91]\n"
    "buffer_load_dwordx4 v216, s[8:11], s38 offen offset:640 lds\n"
    "v_mfma_f32_16x16x32_bf16 v[88:91], a[128:131], v[164:167], v[88:91]\n"
    "ds_read_b64_tr_b16 a[152:153], v224 offset:53248\n"
    "s_add_u32 s37, s36, 286720\n"
    "v_mfma_f32_16x16x32_bf16 v[88:91], a[136:139], v[168:171], v[88:91]\n"
    "ds_read_b64_tr_b16 a[154:155], v224 offset:53760\n"
    "s_add_u32 m0, s45, 77824\n"
    "v_mfma_f32_16x16x32_bf16 v[92:95], a[124:127], v[160:163], v[92:95]\n"
    "ds_read_b128 v[148:151], v231 offset:6144\n"
    "v_mul_f32_e32 v176, v244, v245\n"
    "v_mfma_f32_16x16x32_bf16 v[92:95], a[132:135], v[164:167], v[92:95]\n"
    "buffer_load_dwordx4 v219, s[12:15], s37 offen lds\n"
    "v_mfma_f32_16x16x32_bf16 v[92:95], a[140:143], v[168:171], v[92:95]\n"
    "ds_read_b64_tr_b16 a[160:161], v224 offset:61440\n"
    "s_add_u32 m0, s45, 69632\n"
    "v_mfma_f32_16x16x32_bf16 v[96:99], a[120:123], v[164:167], v[96:99]\n"
    "ds_read_b64_tr_b16 a[162:163], v224 offset:61952\n"
    "v_lshlrev_b32_e32 v244, 16, v204\n"
    "v_mfma_f32_16x16x32_bf16 v[96:99], a[128:131], v[168:171], v[96:99]\n"
    "ds_read_b64_tr_b16 a[156:157], v227 offset:53248\n"
    "v_mul_f32_e32 v244, s51, v244\n"
    "v_mfma_f32_16x16x32_bf16 v[96:99], a[136:139], v[172:175], v[96:99]\n"
    "buffer_load_dwordx4 v217, s[12:15], s37 offen lds\n"
    "v_mfma_f32_16x16x32_bf16 v[100:103], a[124:127], v[164:167], v[100:103]\n"
    "ds_read_b64_tr_b16 a[158:159], v227 offset:53760\n"
    "s_add_u32 m0, s45, 86016\n"
    "v_mfma_f32_16x16x32_bf16 v[100:103], a[132:135], v[168:171], v[100:103]\n"
    "ds_read_b64_tr_b16 a[164:165], v227 offset:61440\n"
    "v_lshlrev_b32_e32 v245, 16, v192\n"
    "v_mfma_f32_16x16x32_bf16 v[100:103], a[140:143], v[172:175], v[100:103]\n"
    "ds_read_b64_tr_b16 a[166:167], v227 offset:61952\n"
    "v_fma_f32 v184, -v245, v176, v244\n"
    "v_mfma_f32_16x16x32_bf16 v[104:107], a[120:123], v[168:171], v[104:107]\n"
    "buffer_load_dwordx4 v221, s[12:15], s37 offen lds\n"
    "v_mfma_f32_16x16x32_bf16 v[104:107], a[128:131], v[172:175], v[104:107]\n"
    "ds_read_b64_tr_b16 a[144:145], v224 offset:45056\n"
    "s_add_u32 m0, s46, 95616\n"
    "v_mfma_f32_16x16x32_bf16 v[108:111], a[124:127], v[168:171], v[108:111]\n"
    "ds_read_b64_tr_b16 a[146:147], v224 offset:45568\n"
    "v_and_b32_e32 v246, 0xffff0000, v196\n"
    "v_mfma_f32_16x16x32_bf16 v[108:111], a[132:135], v[172:175], v[108:111]\n"
    "ds_read_b128 v[152:155], v231 offset:8192\n"
    "v_add_f32_e32 v246, 0x3727c5ac, v246\n"
    "v_mfma_f32_16x16x32_bf16 v[112:115], a[120:123], v[172:175], v[112:115]\n"
    "buffer_load_dwordx4 v216, s[8:11], s39 offen offset:640 lds\n"
    "v_mfma_f32_16x16x32_bf16 v[116:119], a[124:127], v[172:175], v[116:119]\n"
    "ds_read_b64_tr_b16 a[148:149], v227 offset:45056\n"
    "s_waitcnt lgkmcnt(4)\n"
    "v_mfma_f32_16x16x32_bf16 v[80:83], a[152:155], v[144:147], v[80:83]\n"
    "ds_read_b64_tr_b16 a[150:151], v227 offset:45568\n"
    "s_add_u32 m0, s46, 97664\n"
    "v_mfma_f32_16x16x32_bf16 v[80:83], a[160:163], v[148:151], v[80:83]\n"
    "ds_read_b128 v[156:159], v231 offset:10240\n"
    "v_mfma_f32_16x16x32_bf16 v[84:87], a[156:159], v[144:147], v[84:87]\n"
    "buffer_load_dwordx4 v216, s[8:11], s40 offen offset:640 lds\n"
    "v_mfma_f32_16x16x32_bf16 v[84:87], a[164:167], v[148:151], v[84:87]\n"
    "ds_read_b128 v[160:163], v234 offset:4096\n"
    "s_waitcnt lgkmcnt(4)\n"
    "v_mfma_f32_16x16x32_bf16 v[88:91], a[144:147], v[144:147], v[88:91]\n"
    "ds_read_b64_tr_b16 a[176:177], v224 offset:57344\n"
    "s_add_u32 m0, s46, 99712\n"
    "v_mfma_f32_16x16x32_bf16 v[88:91], a[152:155], v[148:151], v[88:91]\n"
    "ds_read_b64_tr_b16 a[178:179], v224 offset:57856\n"
    "v_mfma_f32_16x16x32_bf16 v[88:91], a[160:163], v[152:155], v[88:91]\n"
    "buffer_load_dwordx4 v216, s[8:11], s41 offen offset:640 lds\n"
    "s_waitcnt lgkmcnt(4)\n"
    "v_mfma_f32_16x16x32_bf16 v[92:95], a[148:151], v[144:147], v[92:95]\n"
    "ds_read_b128 v[164:167], v234 offset:6144\n"
    "s_add_u32 m0, s45, 81920\n"
    "v_mfma_f32_16x16x32_bf16 v[92:95], a[156:159], v[148:151], v[92:95]\n"
    "ds_read_b64_tr_b16 a[184:185], v225 offset:0\n"
    "v_mfma_f32_16x16x32_bf16 v[92:95], a[164:167], v[152:155], v[92:95]\n"
    "ds_read_b64_tr_b16 a[186:187], v225 offset:512\n"
    "v_mfma_f32_16x16x32_bf16 v[96:99], a[144:147], v[148:151], v[96:99]\n"
    "buffer_load_dwordx4 v220, s[12:15], s37 offen lds\n"
    "v_mfma_f32_16x16x32_bf16 v[96:99], a[152:155], v[152:155], v[96:99]\n"
    "ds_read_b64_tr_b16 a[180:181], v227 offset:57344\n"
    "s_waitcnt lgkmcnt(4)\n"
    "v_mfma_f32_16x16x32_bf16 v[96:99], a[160:163], v[156:159], v[96:99]\n"
    "ds_read_b64_tr_b16 a[182:183], v227 offset:57856\n"
    "s_add_u32 m0, s45, 73728\n"
    "v_mfma_f32_16x16x32_bf16 v[100:103], a[148:151], v[148:151], v[100:103]\n"
    "ds_read_b64_tr_b16 a[188:189], v228 offset:0\n"
    "v_mfma_f32_16x16x32_bf16 v[100:103], a[156:159], v[152:155], v[100:103]\n"
    "buffer_load_dwordx4 v218, s[12:15], s37 offen lds\n"
    "v_mfma_f32_16x16x32_bf16 v[100:103], a[164:167], v[156:159], v[100:103]\n"
    "ds_read_b64_tr_b16 a[190:191], v228 offset:512\n"
    "s_add_u32 m0, s45, 90112\n"
    "v_mfma_f32_16x16x32_bf16 v[104:107], a[144:147], v[152:155], v[104:107]\n"
    "ds_read_b64_tr_b16 a[168:169], v224 offset:49152\n"
    "v_mfma_f32_16x16x32_bf16 v[104:107], a[152:155], v[156:159], v[104:107]\n"
    "ds_read_b64_tr_b16 a[170:171], v224 offset:49664\n"
    "v_mfma_f32_16x16x32_bf16 v[108:111], a[148:151], v[152:155], v[108:111]\n"
    "buffer_load_dwordx4 v222, s[12:15], s37 offen lds\n"
    "v_mfma_f32_16x16x32_bf16 v[108:111], a[156:159], v[156:159], v[108:111]\n"
    "ds_read_b128 v[168:171], v234 offset:8192\n"
    "v_mfma_f32_16x16x32_bf16 v[112:115], a[144:147], v[156:159], v[112:115]\n"
    "ds_read_b64_tr_b16 a[172:173], v227 offset:49152\n"
    "v_mfma_f32_16x16x32_bf16 v[116:119], a[148:151], v[156:159], v[116:119]\n"
    "ds_read_b64_tr_b16 a[174:175], v227 offset:49664\n"
    "v_mfma_f32_16x16x32_bf16 v[80:83], a[176:179], v[160:163], v[80:83]\n"
    "ds_read_b128 v[172:175], v234 offset:10240\n"
    "s_waitcnt lgkmcnt(5)\n"
    "v_mfma_f32_16x16x32_bf16 v[80:83], a[184:187], v[164:167], v[80:83]\n"
    "v_rsq_f32_e32 v246, v246\n"
    "v_mfma_f32_16x16x32_bf16 v[84:87], a[180:183], v[160:163], v[84:87]\n"
    "v_and_b32_e32 v247, 0xffff0000, v200\n"
    "v_mul_f32_e32 v247, s51, v247\n"
    "v_mfma_f32_16x16x32_bf16 v[84:87], a[188:191], v[164:167], v[84:87]\n"
    "s_waitcnt vmcnt(10) lgkmcnt(0)\n"
    "s_barrier\n"
    "v_mfma_f32_16x16x32_bf16 v[88:91], a[168:171], v[160:163], v[88:91]\n"
    "ds_read_b128 v[144:147], v229 offset:53248\n"
    "s_add_u32 m0, s46, 134400\n"
    "v_mfma_f32_16x16x32_bf16 v[88:91], a[176:179], v[164:167], v[88:91]\n"
    "buffer_load_dwordx4 v216, s[8:11], s38 offen offset:768 lds\n"
    "v_mfma_f32_16x16x32_bf16 v[88:91], a[184:187], v[168:171], v[88:91]\n"
    "ds_read_b64_tr_b16 a[200:201], v223 offset:36864\n"
    "s_add_u32 s37, s36, 344064\n"
    "v_mfma_f32_16x16x32_bf16 v[92:95], a[172:175], v[160:163], v[92:95]\n"
    "ds_read_b64_tr_b16 a[202:203], v223 offset:37376\n"
    "s_add_u32 m0, s45, 118784\n"
    "v_mfma_f32_16x16x32_bf16 v[92:95], a[180:183], v[164:167], v[92:95]\n"
    "ds_read_b128 v[148:151], v229 offset:55296\n"
    "v_mul_f32_e32 v177, v246, v247\n"
    "v_mfma_f32_16x16x32_bf16 v[92:95], a[188:191], v[168:171], v[92:95]\n"
    "buffer_load_dwordx4 v219, s[12:15], s37 offen lds\n"
    "v_mfma_f32_16x16x32_bf16 v[96:99], a[168:171], v[164:167], v[96:99]\n"
    "ds_read_b64_tr_b16 a[208:209], v223 offset:45056\n"
    "s_add_u32 m0, s45, 110592\n"
    "v_mfma_f32_16x16x32_bf16 v[96:99], a[176:179], v[168:171], v[96:99]\n"
    "ds_read_b64_tr_b16 a[210:211], v223 offset:45568\n"
    "v_and_b32_e32 v246, 0xffff0000, v204\n"
    "v_mfma_f32_16x16x32_bf16 v[96:99], a[184:187], v[172:175], v[96:99]\n"
    "ds_read_b64_tr_b16 a[204:205], v226 offset:36864\n"
    "v_mul_f32_e32 v246, s51, v246\n"
    "v_mfma_f32_16x16x32_bf16 v[100:103], a[172:175], v[164:167], v[100:103]\n"
    "buffer_load_dwordx4 v217, s[12:15], s37 offen lds\n"
    "v_mfma_f32_16x16x32_bf16 v[100:103], a[180:183], v[168:171], v[100:103]\n"
    "ds_read_b64_tr_b16 a[206:207], v226 offset:37376\n"
    "s_add_u32 m0, s45, 126976\n"
    "v_mfma_f32_16x16x32_bf16 v[100:103], a[188:191], v[172:175], v[100:103]\n"
    "ds_read_b64_tr_b16 a[212:213], v226 offset:45056\n"
    "v_and_b32_e32 v247, 0xffff0000, v192\n"
    "v_mfma_f32_16x16x32_bf16 v[104:107], a[168:171], v[168:171], v[104:107]\n"
    "ds_read_b64_tr_b16 a[214:215], v226 offset:45568\n"
    "v_fma_f32 v185, -v247, v177, v246\n"
    "v_mfma_f32_16x16x32_bf16 v[104:107], a[176:179], v[172:175], v[104:107]\n"
    "buffer_load_dwordx4 v221, s[12:15], s37 offen lds\n"
    "v_mfma_f32_16x16x32_bf16 v[108:111], a[172:175], v[168:171], v[108:111]\n"
    "ds_read_b64_tr_b16 a[192:193], v223 offset:28672\n"
    "s_add_u32 m0, s46, 136448\n"
    "v_mfma_f32_16x16x32_bf16 v[108:111], a[180:183], v[172:175], v[108:111]\n"
    "ds_read_b64_tr_b16 a[194:195], v223 offset:29184\n"
    "v_lshlrev_b32_e32 v248, 16, v197\n"
    "v_mfma_f32_16x16x32_bf16 v[112:115], a[168:171], v[172:175], v[112:115]\n"
    "ds_read_b128 v[152:155], v229 offset:57344\n"
    "v_add_f32_e32 v248, 0x3727c5ac, v248\n"
    "v_mfma_f32_16x16x32_bf16 v[116:119], a[172:175], v[172:175], v[116:119]\n"
    "buffer_load_dwordx4 v216, s[8:11], s39 offen offset:768 lds\n"
    "ds_read_b64_tr_b16 a[196:197], v226 offset:28672\n"
    "s_waitcnt lgkmcnt(5)\n"
    "v_mfma_f32_16x16x32_bf16 v[80:83], a[200:203], v[144:147], v[80:83]\n"
    "ds_read_b64_tr_b16 a[198:199], v226 offset:29184\n"
    "s_add_u32 m0, s46, 138496\n"
    "v_mfma_f32_16x16x32_bf16 v[80:83], a[208:211], v[148:151], v[80:83]\n"
    "ds_read_b128 v[156:159], v229 offset:59392\n"
    "v_mfma_f32_16x16x32_bf16 v[84:87], a[204:207], v[144:147], v[84:87]\n"
    "ds_read_b128 v[160:163], v232 offset:53248\n"
    "s_waitcnt lgkmcnt(6)\n"
    "v_mfma_f32_16x16x32_bf16 v[84:87], a[212:215], v[148:151], v[84:87]\n"
    "buffer_load_dwordx4 v216, s[8:11], s40 offen offset:768 lds\n"
    "s_waitcnt lgkmcnt(5)\n"
    "v_mfma_f32_16x16x32_bf16 v[88:91], a[192:195], v[144:147], v[88:91]\n"
    "ds_read_b64_tr_b16 a[224:225], v223 offset:40960\n"
    "s_add_u32 m0, s46, 140544\n"
    "v_mfma_f32_16x16x32_bf16 v[88:91], a[200:203], v[148:151], v[88:91]\n"
    "ds_read_b64_tr_b16 a[226:227], v223 offset:41472\n"
    "s_waitcnt lgkmcnt(5)\n"
    "v_mfma_f32_16x16x32_bf16 v[88:91], a[208:211], v[152:155], v[88:91]\n"
    "ds_read_b128 v[164:167], v232 offset:55296\n"
    "s_waitcnt lgkmcnt(5)\n"
    "v_mfma_f32_16x16x32_bf16 v[92:95], a[196:199], v[144:147], v[92:95]\n"
    "buffer_load_dwordx4 v216, s[8:11], s41 offen offset:768 lds\n"
    "v_mfma_f32_16x16x32_bf16 v[92:95], a[204:207], v[148:151], v[92:95]\n"
    "ds_read_b64_tr_b16 a[232:233], v223 offset:49152\n"
    "s_add_u32 m0, s45, 122880\n"
    "v_mfma_f32_16x16x32_bf16 v[92:95], a[212:215], v[152:155], v[92:95]\n"
    "ds_read_b64_tr_b16 a[234:235], v223 offset:49664\n"
    "v_mfma_f32_16x16x32_bf16 v[96:99], a[192:195], v[148:151], v[96:99]\n"
    "ds_read_b64_tr_b16 a[228:229], v226 offset:40960\n"
    "v_mfma_f32_16x16x32_bf16 v[96:99], a[200:203], v[152:155], v[96:99]\n"
    "buffer_load_dwordx4 v220, s[12:15], s37 offen lds\n"
    "s_waitcnt lgkmcnt(4)\n"
    "v_mfma_f32_16x16x32_bf16 v[96:99], a[208:211], v[156:159], v[96:99]\n"
    "ds_read_b64_tr_b16 a[230:231], v226 offset:41472\n"
    "s_add_u32 m0, s45, 114688\n"
    "v_mfma_f32_16x16x32_bf16 v[100:103], a[196:199], v[148:151], v[100:103]\n"
    "ds_read_b64_tr_b16 a[236:237], v226 offset:49152\n"
    "v_mfma_f32_16x16x32_bf16 v[100:103], a[204:207], v[152:155], v[100:103]\n"
    "ds_read_b64_tr_b16 a[238:239], v226 offset:49664\n"
    "v_mfma_f32_16x16x32_bf16 v[100:103], a[212:215], v[156:159], v[100:103]\n"
    "buffer_load_dwordx4 v218, s[12:15], s37 offen lds\n"
    "v_mfma_f32_16x16x32_bf16 v[104:107], a[192:195], v[152:155], v[104:107]\n"
    "ds_read_b64_tr_b16 a[216:217], v223 offset:32768\n"
    "s_add_u32 m0, s45, 131072\n"
    "v_mfma_f32_16x16x32_bf16 v[104:107], a[200:203], v[156:159], v[104:107]\n"
    "ds_read_b64_tr_b16 a[218:219], v223 offset:33280\n"
    "v_mfma_f32_16x16x32_bf16 v[108:111], a[196:199], v[152:155], v[108:111]\n"
    "ds_read_b128 v[168:171], v232 offset:57344\n"
    "v_mfma_f32_16x16x32_bf16 v[108:111], a[204:207], v[156:159], v[108:111]\n"
    "buffer_load_dwordx4 v222, s[12:15], s37 offen lds\n"
    "v_mfma_f32_16x16x32_bf16 v[112:115], a[192:195], v[156:159], v[112:115]\n"
    "ds_read_b64_tr_b16 a[220:221], v226 offset:32768\n"
    "s_add_u32 s44, s48, 3584\n"
    "v_mfma_f32_16x16x32_bf16 v[116:119], a[196:199], v[156:159], v[116:119]\n"
    "ds_read_b64_tr_b16 a[222:223], v226 offset:33280\n"
    "s_add_u32 m0, s47, 0\n"
    "v_mfma_f32_16x16x32_bf16 v[80:83], a[224:227], v[160:163], v[80:83]\n"
    "ds_read_b128 v[172:175], v232 offset:59392\n"
    "s_waitcnt lgkmcnt(5)\n"
    "v_mfma_f32_16x16x32_bf16 v[80:83], a[232:235], v[164:167], v[80:83]\n"
    "buffer_load_dwordx4 v216, s[8:11], s44 offen offset:0 lds\n"
    "v_mfma_f32_16x16x32_bf16 v[84:87], a[228:231], v[160:163], v[84:87]\n"
    "s_add_u32 m0, s47, 1920\n"
    "v_mfma_f32_16x16x32_bf16 v[84:87], a[236:239], v[164:167], v[84:87]\n"
    "s_waitcnt vmcnt(11) lgkmcnt(0)\n"
    "s_barrier\n"
    "v_mfma_f32_16x16x32_bf16 v[88:91], a[216:219], v[160:163], v[88:91]\n"
    "ds_read_b128 v[144:147], v230 offset:28672\n"
    "v_mfma_f32_16x16x32_bf16 v[88:91], a[224:227], v[164:167], v[88:91]\n"
    "buffer_load_dwordx4 v216, s[8:11], s44 offen offset:128 lds\n"
    "v_mfma_f32_16x16x32_bf16 v[88:91], a[232:235], v[168:171], v[88:91]\n"
    "ds_read_b64_tr_b16 a[248:249], v224 offset:12288\n"
    "s_add_u32 m0, s47, 3840\n"
    "v_mfma_f32_16x16x32_bf16 v[92:95], a[220:223], v[160:163], v[92:95]\n"
    "ds_read_b64_tr_b16 a[250:251], v224 offset:12800\n"
    "v_mfma_f32_16x16x32_bf16 v[92:95], a[228:231], v[164:167], v[92:95]\n"
    "ds_read_b128 v[148:151], v230 offset:30720\n"
    "v_mfma_f32_16x16x32_bf16 v[92:95], a[236:239], v[168:171], v[92:95]\n"
    "buffer_load_dwordx4 v216, s[8:11], s44 offen offset:256 lds\n"
    "v_mfma_f32_16x16x32_bf16 v[96:99], a[216:219], v[164:167], v[96:99]\n"
    "ds_read_b64_tr_b16 v[0:1], v224 offset:20480\n"
    "s_add_u32 m0, s47, 5760\n"
    "v_mfma_f32_16x16x32_bf16 v[96:99], a[224:227], v[168:171], v[96:99]\n"
    "ds_read_b64_tr_b16 v[2:3], v224 offset:20992\n"
    "v_mfma_f32_16x16x32_bf16 v[96:99], a[232:235], v[172:175], v[96:99]\n"
    "ds_read_b64_tr_b16 a[252:253], v227 offset:12288\n"
    "v_mfma_f32_16x16x32_bf16 v[100:103], a[220:223], v[164:167], v[100:103]\n"
    "buffer_load_dwordx4 v216, s[8:11], s44 offen offset:384 lds\n"
    "v_mfma_f32_16x16x32_bf16 v[100:103], a[228:231], v[168:171], v[100:103]\n"
    "ds_read_b64_tr_b16 a[254:255], v227 offset:12800\n"
    "s_add_u32 m0, s47, 7680\n"
    "v_mfma_f32_16x16x32_bf16 v[100:103], a[236:239], v[172:175], v[100:103]\n"
    "ds_read_b64_tr_b16 v[4:5], v227 offset:20480\n"
    "v_mfma_f32_16x16x32_bf16 v[104:107], a[216:219], v[168:171], v[104:107]\n"
    "ds_read_b64_tr_b16 v[6:7], v227 offset:20992\n"
    "v_mfma_f32_16x16x32_bf16 v[104:107], a[224:227], v[172:175], v[104:107]\n"
    "buffer_load_dwordx4 v216, s[8:11], s44 offen offset:512 lds\n"
    "v_mfma_f32_16x16x32_bf16 v[108:111], a[220:223], v[168:171], v[108:111]\n"
    "ds_read_b64_tr_b16 a[240:241], v224 offset:4096\n"
    "s_add_u32 m0, s47, 9600\n"
    "v_mfma_f32_16x16x32_bf16 v[108:111], a[228:231], v[172:175], v[108:111]\n"
    "ds_read_b64_tr_b16 a[242:243], v224 offset:4608\n"
    "v_mfma_f32_16x16x32_bf16 v[112:115], a[216:219], v[172:175], v[112:115]\n"
    "ds_read_b128 v[152:155], v230 offset:32768\n"
    "v_mfma_f32_16x16x32_bf16 v[116:119], a[220:223], v[172:175], v[116:119]\n"
    "buffer_load_dwordx4 v216, s[8:11], s44 offen offset:640 lds\n"
    "ds_read_b64_tr_b16 a[244:245], v227 offset:4096\n"
    "s_waitcnt lgkmcnt(5)\n"
    "v_mfma_f32_16x16x32_bf16 v[80:83], a[248:251], v[144:147], v[80:83]\n"
    "ds_read_b64_tr_b16 a[246:247], v227 offset:4608\n"
    "s_add_u32 m0, s47, 11520\n"
    "v_mfma_f32_16x16x32_bf16 v[80:83], v[0:3], v[148:151], v[80:83]\n"
    "ds_read_b128 v[156:159], v230 offset:34816\n"
    "v_mfma_f32_16x16x32_bf16 v[84:87], a[252:255], v[144:147], v[84:87]\n"
    "ds_read_b128 v[160:163], v233 offset:28672\n"
    "s_waitcnt lgkmcnt(6)\n"
    "v_mfma_f32_16x16x32_bf16 v[84:87], v[4:7], v[148:151], v[84:87]\n"
    "buffer_load_dwordx4 v216, s[8:11], s44 offen offset:768 lds\n"
    "s_waitcnt lgkmcnt(5)\n"
    "v_mfma_f32_16x16x32_bf16 v[88:91], a[240:243], v[144:147], v[88:91]\n"
    "ds_read_b64_tr_b16 v[16:17], v224 offset:16384\n"
    "s_add_u32 s44, s48, 4480\n"
    "v_mfma_f32_16x16x32_bf16 v[88:91], a[248:251], v[148:151], v[88:91]\n"
    "ds_read_b64_tr_b16 v[18:19], v224 offset:16896\n"
    "s_waitcnt lgkmcnt(5)\n"
    "v_mfma_f32_16x16x32_bf16 v[88:91], v[0:3], v[152:155], v[88:91]\n"
    "ds_read_b128 v[164:167], v233 offset:30720\n"
    "s_waitcnt lgkmcnt(5)\n"
    "v_mfma_f32_16x16x32_bf16 v[92:95], a[244:247], v[144:147], v[92:95]\n"
    "ds_read_b64_tr_b16 v[24:25], v224 offset:24576\n"
    "s_add_u32 m0, s47, 28672\n"
    "v_mfma_f32_16x16x32_bf16 v[92:95], a[252:255], v[148:151], v[92:95]\n"
    "buffer_load_dwordx4 v216, s[8:11], s44 offen offset:0 lds\n"
    "v_mfma_f32_16x16x32_bf16 v[92:95], v[4:7], v[152:155], v[92:95]\n"
    "ds_read_b64_tr_b16 v[26:27], v224 offset:25088\n"
    "s_add_u32 m0, s47, 30592\n"
    "v_mfma_f32_16x16x32_bf16 v[96:99], a[240:243], v[148:151], v[96:99]\n"
    "ds_read_b64_tr_b16 v[20:21], v227 offset:16384\n"
    "v_mfma_f32_16x16x32_bf16 v[96:99], a[248:251], v[152:155], v[96:99]\n"
    "ds_read_b64_tr_b16 v[22:23], v227 offset:16896\n"
    "s_waitcnt lgkmcnt(5)\n"
    "v_mfma_f32_16x16x32_bf16 v[96:99], v[0:3], v[156:159], v[96:99]\n"
    "buffer_load_dwordx4 v216, s[8:11], s44 offen offset:128 lds\n"
    "v_mfma_f32_16x16x32_bf16 v[100:103], a[244:247], v[148:151], v[100:103]\n"
    "ds_read_b64_tr_b16 v[28:29], v227 offset:24576\n"
    "s_add_u32 m0, s47, 32512\n"
    "v_mfma_f32_16x16x32_bf16 v[100:103], a[252:255], v[152:155], v[100:103]\n"
    "ds_read_b64_tr_b16 v[30:31], v227 offset:25088\n"
    "v_mfma_f32_16x16x32_bf16 v[100:103], v[4:7], v[156:159], v[100:103]\n"
    "ds_read_b64_tr_b16 v[8:9], v224 offset:8192\n"
    "v_mfma_f32_16x16x32_bf16 v[104:107], a[240:243], v[152:155], v[104:107]\n"
    "buffer_load_dwordx4 v216, s[8:11], s44 offen offset:256 lds\n"
    "v_mfma_f32_16x16x32_bf16 v[104:107], a[248:251], v[156:159], v[104:107]\n"
    "ds_read_b64_tr_b16 v[10:11], v224 offset:8704\n"
    "s_add_u32 m0, s47, 34432\n"
    "v_mfma_f32_16x16x32_bf16 v[108:111], a[244:247], v[152:155], v[108:111]\n"
    "ds_read_b128 v[168:171], v233 offset:32768\n"
    "v_mfma_f32_16x16x32_bf16 v[108:111], a[252:255], v[156:159], v[108:111]\n"
    "ds_read_b64_tr_b16 v[12:13], v227 offset:8192\n"
    "v_mfma_f32_16x16x32_bf16 v[112:115], a[240:243], v[156:159], v[112:115]\n"
    "buffer_load_dwordx4 v216, s[8:11], s44 offen offset:384 lds\n"
    "v_mfma_f32_16x16x32_bf16 v[116:119], a[244:247], v[156:159], v[116:119]\n"
    "ds_read_b64_tr_b16 v[14:15], v227 offset:8704\n"
    "s_add_u32 m0, s47, 36352\n"
    "v_mfma_f32_16x16x32_bf16 v[80:83], v[16:19], v[160:163], v[80:83]\n"
    "ds_read_b128 v[172:175], v233 offset:34816\n"
    "s_waitcnt lgkmcnt(5)\n"
    "v_mfma_f32_16x16x32_bf16 v[80:83], v[24:27], v[164:167], v[80:83]\n"
    "v_rsq_f32_e32 v248, v248\n"
    "v_mfma_f32_16x16x32_bf16 v[84:87], v[20:23], v[160:163], v[84:87]\n"
    "buffer_load_dwordx4 v216, s[8:11], s44 offen offset:512 lds\n"
    "v_mfma_f32_16x16x32_bf16 v[84:87], v[28:31], v[164:167], v[84:87]\n"
    "s_waitcnt vmcnt(12) lgkmcnt(0)\n"
    "s_barrier\n"
    "v_mfma_f32_16x16x32_bf16 v[88:91], v[8:11], v[160:163], v[88:91]\n"
    "ds_read_b128 v[144:147], v231 offset:4096\n"
    "s_add_u32 m0, s47, 38272\n"
    "v_mfma_f32_16x16x32_bf16 v[88:91], v[16:19], v[164:167], v[88:91]\n"
    "ds_read_b64_tr_b16 v[40:41], v224 offset:53248\n"
    "v_lshlrev_b32_e32 v249, 16, v201\n"
    "v_mfma_f32_16x16x32_bf16 v[88:91], v[24:27], v[168:171], v[88:91]\n"
    "buffer_load_dwordx4 v216, s[8:11], s44 offen offset:640 lds\n"
    "v_mfma_f32_16x16x32_bf16 v[92:95], v[12:15], v[160:163], v[92:95]\n"
    "ds_read_b64_tr_b16 v[42:43], v224 offset:53760\n"
    "s_add_u32 m0, s47, 40192\n"
    "v_mfma_f32_16x16x32_bf16 v[92:95], v[20:23], v[164:167], v[92:95]\n"
    "ds_read_b128 v[148:151], v231 offset:6144\n"
    "v_mul_f32_e32 v249, s51, v249\n"
    "v_mfma_f32_16x16x32_bf16 v[92:95], v[28:31], v[168:171], v[92:95]\n"
    "ds_read_b64_tr_b16 v[48:49], v224 offset:61440\n"
    "v_mul_f32_e32 v178, v248, v249\n"
    "v_mfma_f32_16x16x32_bf16 v[96:99], v[8:11], v[164:167], v[96:99]\n"
    "buffer_load_dwordx4 v216, s[8:11], s44 offen offset:768 lds\n"
    "v_mfma_f32_16x16x32_bf16 v[96:99], v[16:19], v[168:171], v[96:99]\n"
    "ds_read_b64_tr_b16 v[50:51], v224 offset:61952\n"
    "s_add_u32 s44, s48, 5376\n"
    "v_mfma_f32_16x16x32_bf16 v[96:99], v[24:27], v[172:175], v[96:99]\n"
    "ds_read_b64_tr_b16 v[44:45], v227 offset:53248\n"
    "s_add_u32 m0, s47, 57344\n"
    "v_mfma_f32_16x16x32_bf16 v[100:103], v[12:15], v[164:167], v[100:103]\n"
    "ds_read_b64_tr_b16 v[46:47], v227 offset:53760\n"
    "v_lshlrev_b32_e32 v248, 16, v205\n"
    "v_mfma_f32_16x16x32_bf16 v[100:103], v[20:23], v[168:171], v[100:103]\n"
    "buffer_load_dwordx4 v216, s[8:11], s44 offen offset:0 lds\n"
    "v_mfma_f32_16x16x32_bf16 v[100:103], v[28:31], v[172:175], v[100:103]\n"
    "ds_read_b64_tr_b16 v[52:53], v227 offset:61440\n"
    "s_add_u32 m0, s47, 59264\n"
    "v_mfma_f32_16x16x32_bf16 v[104:107], v[8:11], v[168:171], v[104:107]\n"
    "ds_read_b64_tr_b16 v[54:55], v227 offset:61952\n"
    "v_mul_f32_e32 v248, s51, v248\n"
    "v_mfma_f32_16x16x32_bf16 v[104:107], v[16:19], v[172:175], v[104:107]\n"
    "ds_read_b64_tr_b16 v[32:33], v224 offset:45056\n"
    "v_lshlrev_b32_e32 v249, 16, v193\n"
    "v_mfma_f32_16x16x32_bf16 v[108:111], v[12:15], v[168:171], v[108:111]\n"
    "buffer_load_dwordx4 v216, s[8:11], s44 offen offset:128 lds\n"
    "v_mfma_f32_16x16x32_bf16 v[108:111], v[20:23], v[172:175], v[108:111]\n"
    "ds_read_b64_tr_b16 v[34:35], v224 offset:45568\n"
    "s_add_u32 m0, s47, 61184\n"
    "v_mfma_f32_16x16x32_bf16 v[112:115], v[8:11], v[172:175], v[112:115]\n"
    "ds_read_b128 v[152:155], v231 offset:8192\n"
    "v_fma_f32 v186, -v249, v178, v248\n"
    "v_mfma_f32_16x16x32_bf16 v[116:119], v[12:15], v[172:175], v[116:119]\n"
    "ds_read_b64_tr_b16 v[36:37], v227 offset:45056\n"
    "s_waitcnt lgkmcnt(5)\n"
    "v_mfma_f32_16x16x32_bf16 v[80:83], v[40:43], v[144:147], v[80:83]\n"
    "buffer_load_dwordx4 v216, s[8:11], s44 offen offset:256 lds\n"
    "ds_read_b64_tr_b16 v[38:39], v227 offset:45568\n"
    "v_mfma_f32_16x16x32_bf16 v[80:83], v[48:51], v[148:151], v[80:83]\n"
    "ds_read_b128 v[156:159], v231 offset:10240\n"
    "s_add_u32 m0, s47, 63104\n"
    "v_mfma_f32_16x16x32_bf16 v[84:87], v[44:47], v[144:147], v[84:87]\n"
    "ds_read_b128 v[160:163], v234 offset:4096\n"
    "s_waitcnt lgkmcnt(6)\n"
    "v_mfma_f32_16x16x32_bf16 v[84:87], v[52:55], v[148:151], v[84:87]\n"
    "ds_read_b64_tr_b16 v[64:65], v224 offset:57344\n"
    "s_waitcnt lgkmcnt(6)\n"
    "v_mfma_f32_16x16x32_bf16 v[88:91], v[32:35], v[144:147], v[88:91]\n"
    "buffer_load_dwordx4 v216, s[8:11], s44 offen offset:384 lds\n"
    "v_mfma_f32_16x16x32_bf16 v[88:91], v[40:43], v[148:151], v[88:91]\n"
    "ds_read_b64_tr_b16 v[66:67], v224 offset:57856\n"
    "s_waitcnt lgkmcnt(5)\n"
    "v_mfma_f32_16x16x32_bf16 v[88:91], v[48:51], v[152:155], v[88:91]\n"
    "ds_read_b128 v[164:167], v234 offset:6144\n"
    "s_waitcnt lgkmcnt(5)\n"
    "v_mfma_f32_16x16x32_bf16 v[92:95], v[36:39], v[144:147], v[92:95]\n"
    "ds_read_b64_tr_b16 v[72:73], v225 offset:0\n"
    "s_add_u32 m0, s47, 65024\n"
    "v_mfma_f32_16x16x32_bf16 v[92:95], v[44:47], v[148:151], v[92:95]\n"
    "buffer_load_dwordx4 v216, s[8:11], s44 offen offset:512 lds\n"
    "v_mfma_f32_16x16x32_bf16 v[92:95], v[52:55], v[152:155], v[92:95]\n"
    "ds_read_b64_tr_b16 v[74:75], v225 offset:512\n"
    "s_add_u32 m0, s47, 66944\n"
    "v_mfma_f32_16x16x32_bf16 v[96:99], v[32:35], v[148:151], v[96:99]\n"
    "ds_read_b64_tr_b16 v[68:69], v227 offset:57344\n"
    "v_and_b32_e32 v250, 0xffff0000, v197\n"
    "v_mfma_f32_16x16x32_bf16 v[96:99], v[40:43], v[152:155], v[96:99]\n"
    "ds_read_b64_tr_b16 v[70:71], v227 offset:57856\n"
    "s_waitcnt lgkmcnt(5)\n"
    "v_mfma_f32_16x16x32_bf16 v[96:99], v[48:51], v[156:159], v[96:99]\n"
    "buffer_load_dwordx4 v216, s[8:11], s44 offen offset:640 lds\n"
    "v_mfma_f32_16x16x32_bf16 v[100:103], v[36:39], v[148:151], v[100:103]\n"
    "ds_read_b64_tr_b16 v[76:77], v228 offset:0\n"
    "s_add_u32 m0, s47, 68864\n"
    "v_mfma_f32_16x16x32_bf16 v[100:103], v[44:47], v[152:155], v[100:103]\n"
    "ds_read_b64_tr_b16 v[78:79], v228 offset:512\n"
    "v_add_f32_e32 v250, 0x3727c5ac, v250\n"
    "v_mfma_f32_16x16x32_bf16 v[100:103], v[52:55], v[156:159], v[100:103]\n"
    "ds_read_b64_tr_b16 v[56:57], v224 offset:49152\n"
    "v_mfma_f32_16x16x32_bf16 v[104:107], v[32:35], v[152:155], v[104:107]\n"
    "buffer_load_dwordx4 v216, s[8:11], s44 offen offset:768 lds\n"
    "v_mfma_f32_16x16x32_bf16 v[104:107], v[40:43], v[156:159], v[104:107]\n"
    "ds_read_b64_tr_b16 v[58:59], v224 offset:49664\n"
    "v_mfma_f32_16x16x32_bf16 v[108:111], v[36:39], v[152:155], v[108:111]\n"
    "ds_read_b128 v[168:171], v234 offset:8192\n"
    "v_mfma_f32_16x16x32_bf16 v[108:111], v[44:47], v[156:159], v[108:111]\n"
    "ds_read_b64_tr_b16 v[60:61], v227 offset:49152\n"
    "v_mfma_f32_16x16x32_bf16 v[112:115], v[32:35], v[156:159], v[112:115]\n"
    "ds_read_b64_tr_b16 v[62:63], v227 offset:49664\n"
    "v_mfma_f32_16x16x32_bf16 v[116:119], v[36:39], v[156:159], v[116:119]\n"
    "ds_read_b128 v[172:175], v234 offset:10240\n"
    "v_mfma_f32_16x16x32_bf16 v[80:83], v[64:67], v[160:163], v[80:83]\n"
    "s_waitcnt lgkmcnt(5)\n"
    "v_mfma_f32_16x16x32_bf16 v[80:83], v[72:75], v[164:167], v[80:83]\n"
    "v_rsq_f32_e32 v250, v250\n"
    "v_mfma_f32_16x16x32_bf16 v[84:87], v[68:71], v[160:163], v[84:87]\n"
    "v_and_b32_e32 v251, 0xffff0000, v201\n"
    "v_mul_f32_e32 v251, s51, v251\n"
    "v_mfma_f32_16x16x32_bf16 v[84:87], v[76:79], v[164:167], v[84:87]\n"
    "v_mul_f32_e32 v179, v250, v251\n"
    "s_waitcnt lgkmcnt(2)\n"
    "v_mfma_f32_16x16x32_bf16 v[88:91], v[56:59], v[160:163], v[88:91]\n"
    "s_waitcnt vmcnt(14) lgkmcnt(0)\n"
    "s_barrier\n"
    "v_mfma_f32_16x16x32_bf16 v[88:91], v[64:67], v[164:167], v[88:91]\n"
    "ds_read_b128 v[144:147], v235 offset:0\n"
    "s_add_u32 s44, s48, 6272\n"
    "v_mfma_f32_16x16x32_bf16 v[88:91], v[72:75], v[168:171], v[88:91]\n"
    "ds_read_b128 v[148:151], v238 offset:0\n"
    "s_add_u32 m0, s47, 86016\n"
    "v_mfma_f32_16x16x32_bf16 v[92:95], v[60:63], v[160:163], v[92:95]\n"
    "buffer_load_dwordx4 v216, s[8:11], s44 offen offset:0 lds\n"
    "v_mfma_f32_16x16x32_bf16 v[92:95], v[68:71], v[164:167], v[92:95]\n"
    "ds_read_b128 v[152:155], v235 offset:2048\n"
    "s_add_u32 m0, s47, 87936\n"
    "v_mfma_f32_16x16x32_bf16 v[92:95], v[76:79], v[168:171], v[92:95]\n"
    "ds_read_b128 v[156:159], v238 offset:2048\n"
    "v_and_b32_e32 v250, 0xffff0000, v205\n"
    "v_mfma_f32_16x16x32_bf16 v[96:99], v[56:59], v[164:167], v[96:99]\n"
    "ds_read_b128 v[160:163], v235 offset:4096\n"
    "v_mul_f32_e32 v250, s51, v250\n"
    "v_mfma_f32_16x16x32_bf16 v[96:99], v[64:67], v[168:171], v[96:99]\n"
    "buffer_load_dwordx4 v216, s[8:11], s44 offen offset:128 lds\n"
    "v_mfma_f32_16x16x32_bf16 v[96:99], v[72:75], v[172:175], v[96:99]\n"
    "s_add_u32 m0, s47, 89856\n"
    "v_and_b32_e32 v251, 0xffff0000, v193\n"
    "v_mfma_f32_16x16x32_bf16 v[100:103], v[60:63], v[164:167], v[100:103]\n"
    "ds_read_b128 v[164:167], v238 offset:4096\n"
    "v_fma_f32 v187, -v251, v179, v250\n"
    "v_mfma_f32_16x16x32_bf16 v[100:103], v[68:71], v[168:171], v[100:103]\n"
    "v_lshlrev_b32_e32 v244, 16, v198\n"
    "v_add_f32_e32 v244, 0x3727c5ac, v244\n"
    "v_mfma_f32_16x16x32_bf16 v[100:103], v[76:79], v[172:175], v[100:103]\n"
    "buffer_load_dwordx4 v216, s[8:11], s44 offen offset:256 lds\n"
    "v_mfma_f32_16x16x32_bf16 v[104:107], v[56:59], v[168:171], v[104:107]\n"
    "s_add_u32 m0, s47, 91776\n"
    "v_mfma_f32_16x16x32_bf16 v[104:107], v[64:67], v[172:175], v[104:107]\n"
    "v_rsq_f32_e32 v244, v244\n"
    "v_mfma_f32_16x16x32_bf16 v[108:111], v[60:63], v[168:171], v[108:111]\n"
    "ds_read_b128 v[168:171], v235 offset:6144\n"
    "v_lshlrev_b32_e32 v245, 16, v202\n"
    "v_mfma_f32_16x16x32_bf16 v[108:111], v[68:71], v[172:175], v[108:111]\n"
    "buffer_load_dwordx4 v216, s[8:11], s44 offen offset:384 lds\n"
    "v_mfma_f32_16x16x32_bf16 v[112:115], v[56:59], v[172:175], v[112:115]\n"
    "s_add_u32 m0, s47, 93696\n"
    "v_mul_f32_e32 v245, s51, v245\n"
    "v_mfma_f32_16x16x32_bf16 v[116:119], v[60:63], v[172:175], v[116:119]\n"
    "v_mul_f32_e32 v180, v244, v245\n"
    "s_waitcnt lgkmcnt(1)\n"
    "v_mfma_f32_16x16x32_bf16 v[104:107], a[16:19], v[144:147], v[104:107]\n"
    "v_lshlrev_b32_e32 v244, 16, v206\n"
    "v_mul_f32_e32 v244, s51, v244\n"
    "v_mfma_f32_16x16x32_bf16 v[104:107], a[40:43], v[148:151], v[104:107]\n"
    "v_lshlrev_b32_e32 v245, 16, v194\n"
    "v_fma_f32 v188, -v245, v180, v244\n"
    "v_mfma_f32_16x16x32_bf16 v[104:107], a[64:67], v[152:155], v[104:107]\n"
    "v_and_b32_e32 v246, 0xffff0000, v198\n"
    "v_mfma_f32_16x16x32_bf16 v[104:107], a[88:91], v[156:159], v[104:107]\n"
    "v_add_f32_e32 v246, 0x3727c5ac, v246\n"
    "v_mfma_f32_16x16x32_bf16 v[104:107], a[112:115], v[160:163], v[104:107]\n"
    "v_rsq_f32_e32 v246, v246\n"
    "v_mfma_f32_16x16x32_bf16 v[104:107], a[136:139], v[164:167], v[104:107]\n"
    "v_and_b32_e32 v247, 0xffff0000, v202\n"
    "s_waitcnt lgkmcnt(0)\n"
    "v_mfma_f32_16x16x32_bf16 v[104:107], a[160:163], v[168:171], v[104:107]\n"
    "buffer_load_dwordx4 v216, s[8:11], s44 offen offset:512 lds\n"
    "v_mfma_f32_16x16x32_bf16 v[108:111], a[20:23], v[144:147], v[108:111]\n"
    "s_add_u32 m0, s47, 95616\n"
    "v_mul_f32_e32 v247, s51, v247\n"
    "v_mfma_f32_16x16x32_bf16 v[108:111], a[44:47], v[148:151], v[108:111]\n"
    "v_mul_f32_e32 v181, v246, v247\n"
    "v_and_b32_e32 v246, 0xffff0000, v206\n"
    "v_mfma_f32_16x16x32_bf16 v[108:111], a[68:71], v[152:155], v[108:111]\n"
    "v_mul_f32_e32 v246, s51, v246\n"
    "v_and_b32_e32 v247, 0xffff0000, v194\n"
    "v_mfma_f32_16x16x32_bf16 v[108:111], a[92:95], v[156:159], v[108:111]\n"
    "v_fma_f32 v189, -v247, v181, v246\n"
    "v_mfma_f32_16x16x32_bf16 v[108:111], a[116:119], v[160:163], v[108:111]\n"
    "v_lshlrev_b32_e32 v248, 16, v199\n"
    "v_add_f32_e32 v248, 0x3727c5ac, v248\n"
    "v_mfma_f32_16x16x32_bf16 v[108:111], a[140:143], v[164:167], v[108:111]\n"
    "v_rsq_f32_e32 v248, v248\n"
    "v_mfma_f32_16x16x32_bf16 v[108:111], a[164:167], v[168:171], v[108:111]\n"
    "v_lshlrev_b32_e32 v249, 16, v203\n"
    "v_mul_f32_e32 v249, s51, v249\n"
    "v_mfma_f32_16x16x32_bf16 v[112:115], a[8:11], v[144:147], v[112:115]\n"
    "buffer_load_dwordx4 v216, s[8:11], s44 offen offset:640 lds\n"
    "v_mfma_f32_16x16x32_bf16 v[112:115], a[32:35], v[148:151], v[112:115]\n"
    "s_add_u32 m0, s47, 97536\n"
    "v_mul_f32_e32 v182, v248, v249\n"
    "v_mfma_f32_16x16x32_bf16 v[112:115], a[56:59], v[152:155], v[112:115]\n"
    "v_lshlrev_b32_e32 v248, 16, v207\n"
    "v_mul_f32_e32 v248, s51, v248\n"
    "v_mfma_f32_16x16x32_bf16 v[112:115], a[80:83], v[156:159], v[112:115]\n"
    "v_lshlrev_b32_e32 v249, 16, v195\n"
    "v_fma_f32 v190, -v249, v182, v248\n"
    "v_mfma_f32_16x16x32_bf16 v[112:115], a[104:107], v[160:163], v[112:115]\n"
    "v_and_b32_e32 v250, 0xffff0000, v199\n"
    "v_mfma_f32_16x16x32_bf16 v[112:115], a[128:131], v[164:167], v[112:115]\n"
    "v_add_f32_e32 v250, 0x3727c5ac, v250\n"
    "v_mfma_f32_16x16x32_bf16 v[112:115], a[152:155], v[168:171], v[112:115]\n"
    "v_rsq_f32_e32 v250, v250\n"
    "v_mfma_f32_16x16x32_bf16 v[116:119], a[12:15], v[144:147], v[116:119]\n"
    "v_and_b32_e32 v251, 0xffff0000, v203\n"
    "v_mul_f32_e32 v251, s51, v251\n"
    "v_mfma_f32_16x16x32_bf16 v[116:119], a[36:39], v[148:151], v[116:119]\n"
    "buffer_load_dwordx4 v216, s[8:11], s44 offen offset:768 lds\n"
    "v_mfma_f32_16x16x32_bf16 v[116:119], a[60:63], v[152:155], v[116:119]\n"
    "v_mul_f32_e32 v183, v250, v251\n"
    "v_and_b32_e32 v250, 0xffff0000, v207\n"
    "v_mfma_f32_16x16x32_bf16 v[116:119], a[84:87], v[156:159], v[116:119]\n"
    "ds_read_b128 v[172:175], v238 offset:6144\n"
    "v_mul_f32_e32 v250, s51, v250\n"
    "v_mfma_f32_16x16x32_bf16 v[116:119], a[108:111], v[160:163], v[116:119]\n"
    "v_and_b32_e32 v251, 0xffff0000, v195\n"
    "v_fma_f32 v191, -v251, v183, v250\n"
    "v_mfma_f32_16x16x32_bf16 v[116:119], a[132:135], v[164:167], v[116:119]\n"
    "v_fma_f32 v80, v80, v176, v184\n"
    "v_fma_f32 v81, v81, v177, v185\n"
    "v_mfma_f32_16x16x32_bf16 v[116:119], a[156:159], v[168:171], v[116:119]\n"
    "v_exp_f32_e32 v192, v80\n"
    "v_mfma_f32_16x16x32_bf16 v[120:123], a[0:3], v[144:147], 0\n"
    "v_exp_f32_e32 v193, v81\n"
    "v_mfma_f32_16x16x32_bf16 v[120:123], a[24:27], v[148:151], v[120:123]\n"
    "v_fma_f32 v192, v192, s51, s51\n"
    "v_fma_f32 v193, v193, s51, s51\n"
    "v_mfma_f32_16x16x32_bf16 v[120:123], a[48:51], v[152:155], v[120:123]\n"
    "v_rcp_f32_e32 v192, v192\n"
    "v_mfma_f32_16x16x32_bf16 v[120:123], a[72:75], v[156:159], v[120:123]\n"
    "v_rcp_f32_e32 v193, v193\n"
    "v_mfma_f32_16x16x32_bf16 v[120:123], a[96:99], v[160:163], v[120:123]\n"
    "v_mul_f32_e32 v80, v80, v192\n"
    "v_mul_f32_e32 v81, v81, v193\n"
    "v_mfma_f32_16x16x32_bf16 v[120:123], a[120:123], v[164:167], v[120:123]\n"
    "v_cvt_pk_bf16_f32 v200, v80, v81\n"
    "v_fma_f32 v82, v82, v178, v186\n"
    "v_mfma_f32_16x16x32_bf16 v[120:123], a[144:147], v[168:171], v[120:123]\n"
    "v_fma_f32 v83, v83, v179, v187\n"
    "v_mfma_f32_16x16x32_bf16 v[124:127], a[4:7], v[144:147], 0\n"
    "ds_read_b128 v[144:147], v235 offset:8192\n"
    "v_fma_f32 v84, v84, v180, v188\n"
    "v_mfma_f32_16x16x32_bf16 v[124:127], a[28:31], v[148:151], v[124:127]\n"
    "ds_read_b128 v[148:151], v238 offset:8192\n"
    "v_fma_f32 v85, v85, v181, v189\n"
    "v_mfma_f32_16x16x32_bf16 v[124:127], a[52:55], v[152:155], v[124:127]\n"
    "ds_read_b128 v[152:155], v235 offset:10240\n"
    "v_fma_f32 v86, v86, v182, v190\n"
    "v_mfma_f32_16x16x32_bf16 v[124:127], a[76:79], v[156:159], v[124:127]\n"
    "ds_read_b128 v[156:159], v238 offset:10240\n"
    "v_fma_f32 v87, v87, v183, v191\n"
    "v_mfma_f32_16x16x32_bf16 v[124:127], a[100:103], v[160:163], v[124:127]\n"
    "ds_read_b128 v[160:163], v235 offset:12288\n"
    "v_mfma_f32_16x16x32_bf16 v[124:127], a[124:127], v[164:167], v[124:127]\n"
    "ds_read_b128 v[164:167], v238 offset:12288\n"
    "v_mfma_f32_16x16x32_bf16 v[124:127], a[148:151], v[168:171], v[124:127]\n"
    "s_waitcnt lgkmcnt(5)\n"
    "v_mfma_f32_16x16x32_bf16 v[104:107], a[184:187], v[172:175], v[104:107]\n"
    "v_exp_f32_e32 v194, v82\n"
    "v_mfma_f32_16x16x32_bf16 v[104:107], a[208:211], v[144:147], v[104:107]\n"
    "v_fma_f32 v194, v194, s51, s51\n"
    "s_waitcnt lgkmcnt(3)\n"
    "v_mfma_f32_16x16x32_bf16 v[104:107], a[232:235], v[148:151], v[104:107]\n"
    "v_exp_f32_e32 v195, v83\n"
    "v_mfma_f32_16x16x32_bf16 v[104:107], v[0:3], v[152:155], v[104:107]\n"
    "v_fma_f32 v195, v195, s51, s51\n"
    "s_waitcnt lgkmcnt(1)\n"
    "v_mfma_f32_16x16x32_bf16 v[104:107], v[24:27], v[156:159], v[104:107]\n"
    "v_rcp_f32_e32 v194, v194\n"
    "v_mfma_f32_16x16x32_bf16 v[104:107], v[48:51], v[160:163], v[104:107]\n"
    "v_mul_f32_e32 v82, v82, v194\n"
    "s_waitcnt lgkmcnt(0)\n"
    "v_mfma_f32_16x16x32_bf16 v[104:107], v[72:75], v[164:167], v[104:107]\n"
    "v_rcp_f32_e32 v195, v195\n"
    "v_mfma_f32_16x16x32_bf16 v[108:111], a[188:191], v[172:175], v[108:111]\n"
    "v_mul_f32_e32 v83, v83, v195\n"
    "v_cvt_pk_bf16_f32 v201, v82, v83\n"
    "v_mfma_f32_16x16x32_bf16 v[108:111], a[212:215], v[144:147], v[108:111]\n"
    "v_exp_f32_e32 v196, v84\n"
    "v_mfma_f32_16x16x32_bf16 v[108:111], a[236:239], v[148:151], v[108:111]\n"
    "v_exp_f32_e32 v197, v85\n"
    "v_mfma_f32_16x16x32_bf16 v[108:111], v[4:7], v[152:155], v[108:111]\n"
    "v_fma_f32 v196, v196, s51, s51\n"
    "v_fma_f32 v197, v197, s51, s51\n"
    "v_mfma_f32_16x16x32_bf16 v[108:111], v[28:31], v[156:159], v[108:111]\n"
    "v_rcp_f32_e32 v196, v196\n"
    "v_mfma_f32_16x16x32_bf16 v[108:111], v[52:55], v[160:163], v[108:111]\n"
    "v_rcp_f32_e32 v197, v197\n"
    "v_mfma_f32_16x16x32_bf16 v[108:111], v[76:79], v[164:167], v[108:111]\n"
    "v_mul_f32_e32 v84, v84, v196\n"
    "v_mul_f32_e32 v85, v85, v197\n"
    "v_mfma_f32_16x16x32_bf16 v[112:115], a[176:179], v[172:175], v[112:115]\n"
    "v_cvt_pk_bf16_f32 v202, v84, v85\n"
    "v_fma_f32 v88, v88, v176, v184\n"
    "v_mfma_f32_16x16x32_bf16 v[112:115], a[200:203], v[144:147], v[112:115]\n"
    "v_exp_f32_e32 v198, v86\n"
    "v_mfma_f32_16x16x32_bf16 v[112:115], a[224:227], v[148:151], v[112:115]\n"
    "v_exp_f32_e32 v199, v87\n"
    "v_mfma_f32_16x16x32_bf16 v[112:115], a[248:251], v[152:155], v[112:115]\n"
    "v_fma_f32 v198, v198, s51, s51\n"
    "v_fma_f32 v199, v199, s51, s51\n"
    "v_mfma_f32_16x16x32_bf16 v[112:115], v[16:19], v[156:159], v[112:115]\n"
    "v_rcp_f32_e32 v198, v198\n"
    "v_mfma_f32_16x16x32_bf16 v[112:115], v[40:43], v[160:163], v[112:115]\n"
    "v_rcp_f32_e32 v199, v199\n"
    "v_mfma_f32_16x16x32_bf16 v[112:115], v[64:67], v[164:167], v[112:115]\n"
    "v_mul_f32_e32 v86, v86, v198\n"
    "v_mul_f32_e32 v87, v87, v199\n"
    "v_mfma_f32_16x16x32_bf16 v[116:119], a[180:183], v[172:175], v[116:119]\n"
    "v_cvt_pk_bf16_f32 v203, v86, v87\n"
    "v_fma_f32 v89, v89, v177, v185\n"
    "v_mfma_f32_16x16x32_bf16 v[116:119], a[204:207], v[144:147], v[116:119]\n"
    "v_exp_f32_e32 v192, v88\n"
    "v_mfma_f32_16x16x32_bf16 v[116:119], a[228:231], v[148:151], v[116:119]\n"
    "v_exp_f32_e32 v193, v89\n"
    "v_mfma_f32_16x16x32_bf16 v[116:119], a[252:255], v[152:155], v[116:119]\n"
    "s_waitcnt vmcnt(14)\n"
    "s_barrier\n"
    "v_mfma_f32_16x16x32_bf16 v[116:119], v[20:23], v[156:159], v[116:119]\n"
    "ds_read_b128 v[168:171], v235 offset:28672\n"
    "s_add_u32 s44, s48, 7168\n"
    "v_mfma_f32_16x16x32_bf16 v[116:119], v[44:47], v[160:163], v[116:119]\n"
    "ds_write_b128 v241, v[200:203] offset:0\n"
    "v_mfma_f32_16x16x32_bf16 v[116:119], v[68:71], v[164:167], v[116:119]\n"
    "s_add_u32 m0, s47, 0\n"
    "v_fma_f32 v192, v192, s51, s51\n"
    "v_mfma_f32_16x16x32_bf16 v[120:123], a[168:171], v[172:175], v[120:123]\n"
    "buffer_load_dwordx4 v216, s[8:11], s44 offen offset:0 lds\n"
    "v_mfma_f32_16x16x32_bf16 v[120:123], a[192:195], v[144:147], v[120:123]\n"
    "s_add_u32 m0, s47, 1920\n"
    "v_fma_f32 v193, v193, s51, s51\n"
    "v_mfma_f32_16x16x32_bf16 v[120:123], a[216:219], v[148:151], v[120:123]\n"
    "v_rcp_f32_e32 v192, v192\n"
    "v_mfma_f32_16x16x32_bf16 v[120:123], a[240:243], v[152:155], v[120:123]\n"
    "v_rcp_f32_e32 v193, v193\n"
    "v_mfma_f32_16x16x32_bf16 v[120:123], v[8:11], v[156:159], v[120:123]\n"
    "v_mul_f32_e32 v88, v88, v192\n"
    "v_mul_f32_e32 v89, v89, v193\n"
    "v_mfma_f32_16x16x32_bf16 v[120:123], v[32:35], v[160:163], v[120:123]\n"
    "v_cvt_pk_bf16_f32 v204, v88, v89\n"
    "v_fma_f32 v90, v90, v178, v186\n"
    "v_mfma_f32_16x16x32_bf16 v[120:123], v[56:59], v[164:167], v[120:123]\n"
    "v_fma_f32 v91, v91, v179, v187\n"
    "v_mfma_f32_16x16x32_bf16 v[124:127], a[172:175], v[172:175], v[124:127]\n"
    "ds_read_b128 v[172:175], v238 offset:28672\n"
    "v_fma_f32 v92, v92, v180, v188\n"
    "v_mfma_f32_16x16x32_bf16 v[124:127], a[196:199], v[144:147], v[124:127]\n"
    "ds_read_b128 v[144:147], v235 offset:30720\n"
    "v_fma_f32 v93, v93, v181, v189\n"
    "v_mfma_f32_16x16x32_bf16 v[124:127], a[220:223], v[148:151], v[124:127]\n"
    "ds_read_b128 v[148:151], v238 offset:30720\n"
    "v_fma_f32 v94, v94, v182, v190\n"
    "v_mfma_f32_16x16x32_bf16 v[124:127], a[244:247], v[152:155], v[124:127]\n"
    "ds_read_b128 v[152:155], v235 offset:32768\n"
    "v_fma_f32 v95, v95, v183, v191\n"
    "v_mfma_f32_16x16x32_bf16 v[124:127], v[12:15], v[156:159], v[124:127]\n"
    "ds_read_b128 v[156:159], v238 offset:32768\n"
    "v_mfma_f32_16x16x32_bf16 v[124:127], v[36:39], v[160:163], v[124:127]\n"
    "ds_read_b128 v[160:163], v235 offset:34816\n"
    "v_mfma_f32_16x16x32_bf16 v[124:127], v[60:63], v[164:167], v[124:127]\n"
    "s_waitcnt lgkmcnt(5)\n"
    "v_mfma_f32_16x16x32_bf16 v[112:115], a[16:19], v[168:171], v[112:115]\n"
    "buffer_load_dwordx4 v216, s[8:11], s44 offen offset:128 lds\n"
    "v_mfma_f32_16x16x32_bf16 v[112:115], a[40:43], v[172:175], v[112:115]\n"
    "s_add_u32 m0, s47, 3840\n"
    "s_waitcnt lgkmcnt(3)\n"
    "v_mfma_f32_16x16x32_bf16 v[112:115], a[64:67], v[144:147], v[112:115]\n"
    "v_exp_f32_e32 v194, v90\n"
    "v_mfma_f32_16x16x32_bf16 v[112:115], a[88:91], v[148:151], v[112:115]\n"
    "v_fma_f32 v194, v194, s51, s51\n"
    "s_waitcnt lgkmcnt(1)\n"
    "v_mfma_f32_16x16x32_bf16 v[112:115], a[112:115], v[152:155], v[112:115]\n"
    "v_exp_f32_e32 v195, v91\n"
    "v_mfma_f32_16x16x32_bf16 v[112:115], a[136:139], v[156:159], v[112:115]\n"
    "v_fma_f32 v195, v195, s51, s51\n"
    "s_waitcnt lgkmcnt(0)\n"
    "v_mfma_f32_16x16x32_bf16 v[112:115], a[160:163], v[160:163], v[112:115]\n"
    "v_rcp_f32_e32 v194, v194\n"
    "v_mfma_f32_16x16x32_bf16 v[116:119], a[20:23], v[168:171], v[116:119]\n"
    "v_rcp_f32_e32 v195, v195\n"
    "v_mfma_f32_16x16x32_bf16 v[116:119], a[44:47], v[172:175], v[116:119]\n"
    "buffer_load_dwordx4 v216, s[8:11], s44 offen offset:256 lds\n"
    "v_mfma_f32_16x16x32_bf16 v[116:119], a[68:71], v[144:147], v[116:119]\n"
    "s_add_u32 m0, s47, 5760\n"
    "v_mul_f32_e32 v90, v90, v194\n"
    "v_mfma_f32_16x16x32_bf16 v[116:119], a[92:95], v[148:151], v[116:119]\n"
    "v_mul_f32_e32 v91, v91, v195\n"
    "v_cvt_pk_bf16_f32 v205, v90, v91\n"
    "v_mfma_f32_16x16x32_bf16 v[116:119], a[116:119], v[152:155], v[116:119]\n"
    "v_exp_f32_e32 v196, v92\n"
    "v_mfma_f32_16x16x32_bf16 v[116:119], a[140:143], v[156:159], v[116:119]\n"
    "v_exp_f32_e32 v197, v93\n"
    "v_mfma_f32_16x16x32_bf16 v[116:119], a[164:167], v[160:163], v[116:119]\n"
    "v_fma_f32 v196, v196, s51, s51\n"
    "v_fma_f32 v197, v197, s51, s51\n"
    "v_mfma_f32_16x16x32_bf16 v[120:123], a[8:11], v[168:171], v[120:123]\n"
    "v_rcp_f32_e32 v196, v196\n"
    "v_mfma_f32_16x16x32_bf16 v[120:123], a[32:35], v[172:175], v[120:123]\n"
    "v_rcp_f32_e32 v197, v197\n"
    "v_mfma_f32_16x16x32_bf16 v[120:123], a[56:59], v[144:147], v[120:123]\n"
    "buffer_load_dwordx4 v216, s[8:11], s44 offen offset:384 lds\n"
    "v_mfma_f32_16x16x32_bf16 v[120:123], a[80:83], v[148:151], v[120:123]\n"
    "s_add_u32 m0, s47, 7680\n"
    "v_mul_f32_e32 v92, v92, v196\n"
    "v_mfma_f32_16x16x32_bf16 v[120:123], a[104:107], v[152:155], v[120:123]\n"
    "v_mul_f32_e32 v93, v93, v197\n"
    "v_cvt_pk_bf16_f32 v206, v92, v93\n"
    "v_mfma_f32_16x16x32_bf16 v[120:123], a[128:131], v[156:159], v[120:123]\n"
    "v_exp_f32_e32 v198, v94\n"
    "v_mfma_f32_16x16x32_bf16 v[120:123], a[152:155], v[160:163], v[120:123]\n"
    "v_exp_f32_e32 v199, v95\n"
    "v_mfma_f32_16x16x32_bf16 v[124:127], a[12:15], v[168:171], v[124:127]\n"
    "v_fma_f32 v198, v198, s51, s51\n"
    "v_fma_f32 v199, v199, s51, s51\n"
    "v_mfma_f32_16x16x32_bf16 v[124:127], a[36:39], v[172:175], v[124:127]\n"
    "v_rcp_f32_e32 v198, v198\n"
    "v_mfma_f32_16x16x32_bf16 v[124:127], a[60:63], v[144:147], v[124:127]\n"
    "v_rcp_f32_e32 v199, v199\n"
    "v_mfma_f32_16x16x32_bf16 v[124:127], a[84:87], v[148:151], v[124:127]\n"
    "buffer_load_dwordx4 v216, s[8:11], s44 offen offset:512 lds\n"
    "v_mfma_f32_16x16x32_bf16 v[124:127], a[108:111], v[152:155], v[124:127]\n"
    "ds_read_b128 v[164:167], v238 offset:34816\n"
    "s_add_u32 m0, s47, 9600\n"
    "v_mfma_f32_16x16x32_bf16 v[124:127], a[132:135], v[156:159], v[124:127]\n"
    "v_mul_f32_e32 v94, v94, v198\n"
    "v_mul_f32_e32 v95, v95, v199\n"
    "v_mfma_f32_16x16x32_bf16 v[124:127], a[156:159], v[160:163], v[124:127]\n"
    "v_cvt_pk_bf16_f32 v207, v94, v95\n"
    "v_fma_f32 v96, v96, v176, v184\n"
    "v_mfma_f32_16x16x32_bf16 v[128:131], a[0:3], v[168:171], 0\n"
    "ds_write_b128 v241, v[204:207] offset:4096\n"
    "v_mfma_f32_16x16x32_bf16 v[128:131], a[24:27], v[172:175], v[128:131]\n"
    "v_fma_f32 v97, v97, v177, v185\n"
    "v_fma_f32 v98, v98, v178, v186\n"
    "v_mfma_f32_16x16x32_bf16 v[128:131], a[48:51], v[144:147], v[128:131]\n"
    "v_exp_f32_e32 v192, v96\n"
    "v_mfma_f32_16x16x32_bf16 v[128:131], a[72:75], v[148:151], v[128:131]\n"
    "v_exp_f32_e32 v193, v97\n"
    "v_mfma_f32_16x16x32_bf16 v[128:131], a[96:99], v[152:155], v[128:131]\n"
    "buffer_load_dwordx4 v216, s[8:11], s44 offen offset:640 lds\n"
    "v_mfma_f32_16x16x32_bf16 v[128:131], a[120:123], v[156:159], v[128:131]\n"
    "s_add_u32 m0, s47, 11520\n"
    "v_fma_f32 v192, v192, s51, s51\n"
    "v_mfma_f32_16x16x32_bf16 v[128:131], a[144:147], v[160:163], v[128:131]\n"
    "v_fma_f32 v193, v193, s51, s51\n"
    "v_fma_f32 v99, v99, v179, v187\n"
    "v_mfma_f32_16x16x32_bf16 v[132:135], a[4:7], v[168:171], 0\n"
    "ds_read_b128 v[168:171], v235 offset:36864\n"
    "v_fma_f32 v100, v100, v180, v188\n"
    "v_mfma_f32_16x16x32_bf16 v[132:135], a[28:31], v[172:175], v[132:135]\n"
    "ds_read_b128 v[172:175], v238 offset:36864\n"
    "v_fma_f32 v101, v101, v181, v189\n"
    "v_mfma_f32_16x16x32_bf16 v[132:135], a[52:55], v[144:147], v[132:135]\n"
    "ds_read_b128 v[144:147], v235 offset:38912\n"
    "v_fma_f32 v102, v102, v182, v190\n"
    "v_mfma_f32_16x16x32_bf16 v[132:135], a[76:79], v[148:151], v[132:135]\n"
    "ds_read_b128 v[148:151], v238 offset:38912\n"
    "v_fma_f32 v103, v103, v183, v191\n"
    "v_mfma_f32_16x16x32_bf16 v[132:135], a[100:103], v[152:155], v[132:135]\n"
    "ds_read_b128 v[152:155], v235 offset:40960\n"
    "v_mfma_f32_16x16x32_bf16 v[132:135], a[124:127], v[156:159], v[132:135]\n"
    "ds_read_b128 v[156:159], v238 offset:40960\n"
    "v_mfma_f32_16x16x32_bf16 v[132:135], a[148:151], v[160:163], v[132:135]\n"
    "s_waitcnt lgkmcnt(5)\n"
    "v_mfma_f32_16x16x32_bf16 v[112:115], a[184:187], v[164:167], v[112:115]\n"
    "buffer_load_dwordx4 v216, s[8:11], s44 offen offset:768 lds\n"
    "v_mfma_f32_16x16x32_bf16 v[112:115], a[208:211], v[168:171], v[112:115]\n"
    "s_waitcnt lgkmcnt(3)\n"
    "v_mfma_f32_16x16x32_bf16 v[112:115], a[232:235], v[172:175], v[112:115]\n"
    "v_rcp_f32_e32 v192, v192\n"
    "v_mfma_f32_16x16x32_bf16 v[112:115], v[0:3], v[144:147], v[112:115]\n"
    "v_mul_f32_e32 v96, v96, v192\n"
    "s_waitcnt lgkmcnt(1)\n"
    "v_mfma_f32_16x16x32_bf16 v[112:115], v[24:27], v[148:151], v[112:115]\n"
    "v_rcp_f32_e32 v193, v193\n"
    "v_mfma_f32_16x16x32_bf16 v[112:115], v[48:51], v[152:155], v[112:115]\n"
    "v_mul_f32_e32 v97, v97, v193\n"
    "s_waitcnt lgkmcnt(0)\n"
    "v_mfma_f32_16x16x32_bf16 v[112:115], v[72:75], v[156:159], v[112:115]\n"
    "v_cvt_pk_bf16_f32 v200, v96, v97\n"
    "v_mfma_f32_16x16x32_bf16 v[116:119], a[188:191], v[164:167], v[116:119]\n"
    "v_exp_f32_e32 v194, v98\n"
    "v_mfma_f32_16x16x32_bf16 v[116:119], a[212:215], v[168:171], v[116:119]\n"
    "v_exp_f32_e32 v195, v99\n"
    "v_mfma_f32_16x16x32_bf16 v[116:119], a[236:239], v[172:175], v[116:119]\n"
    "v_fma_f32 v194, v194, s51, s51\n"
    "v_fma_f32 v195, v195, s51, s51\n"
    "v_mfma_f32_16x16x32_bf16 v[116:119], v[4:7], v[144:147], v[116:119]\n"
    "v_rcp_f32_e32 v194, v194\n"
    "v_mfma_f32_16x16x32_bf16 v[116:119], v[28:31], v[148:151], v[116:119]\n"
    "v_rcp_f32_e32 v195, v195\n"
    "v_mfma_f32_16x16x32_bf16 v[116:119], v[52:55], v[152:155], v[116:119]\n"
    "v_mul_f32_e32 v98, v98, v194\n"
    "v_mul_f32_e32 v99, v99, v195\n"
    "v_mfma_f32_16x16x32_bf16 v[116:119], v[76:79], v[156:159], v[116:119]\n"
    "v_cvt_pk_bf16_f32 v201, v98, v99\n"
    "v_fma_f32 v104, v104, v176, v184\n"
    "v_mfma_f32_16x16x32_bf16 v[120:123], a[176:179], v[164:167], v[120:123]\n"
    "v_exp_f32_e32 v196, v100\n"
    "v_mfma_f32_16x16x32_bf16 v[120:123], a[200:203], v[168:171], v[120:123]\n"
    "v_exp_f32_e32 v197, v101\n"
    "v_mfma_f32_16x16x32_bf16 v[120:123], a[224:227], v[172:175], v[120:123]\n"
    "v_fma_f32 v196, v196, s51, s51\n"
    "v_fma_f32 v197, v197, s51, s51\n"
    "v_mfma_f32_16x16x32_bf16 v[120:123], a[248:251], v[144:147], v[120:123]\n"
    "v_rcp_f32_e32 v196, v196\n"
    "v_mfma_f32_16x16x32_bf16 v[120:123], v[16:19], v[148:151], v[120:123]\n"
    "v_rcp_f32_e32 v197, v197\n"
    "v_mfma_f32_16x16x32_bf16 v[120:123], v[40:43], v[152:155], v[120:123]\n"
    "v_mul_f32_e32 v100, v100, v196\n"
    "v_mul_f32_e32 v101, v101, v197\n"
    "v_mfma_f32_16x16x32_bf16 v[120:123], v[64:67], v[156:159], v[120:123]\n"
    "v_cvt_pk_bf16_f32 v202, v100, v101\n"
    "v_fma_f32 v105, v105, v177, v185\n"
    "v_mfma_f32_16x16x32_bf16 v[124:127], a[180:183], v[164:167], v[124:127]\n"
    "v_exp_f32_e32 v198, v102\n"
    "v_mfma_f32_16x16x32_bf16 v[124:127], a[204:207], v[168:171], v[124:127]\n"
    "v_exp_f32_e32 v199, v103\n"
    "v_mfma_f32_16x16x32_bf16 v[124:127], a[228:231], v[172:175], v[124:127]\n"
    "v_fma_f32 v198, v198, s51, s51\n"
    "v_fma_f32 v199, v199, s51, s51\n"
    "v_mfma_f32_16x16x32_bf16 v[124:127], a[252:255], v[144:147], v[124:127]\n"
    "s_waitcnt vmcnt(14)\n"
    "s_barrier\n"
    "v_mfma_f32_16x16x32_bf16 v[124:127], v[20:23], v[148:151], v[124:127]\n"
    "ds_read_b128 v[160:163], v235 offset:57344\n"
    "s_add_u32 s44, s48, 8064\n"
    "v_mfma_f32_16x16x32_bf16 v[124:127], v[44:47], v[152:155], v[124:127]\n"
    "ds_read_b128 v[208:211], v242 offset:0\n"
    "s_add_u32 m0, s47, 28672\n"
    "v_mfma_f32_16x16x32_bf16 v[124:127], v[68:71], v[156:159], v[124:127]\n"
    "buffer_load_dwordx4 v216, s[8:11], s44 offen offset:0 lds\n"
    "v_mfma_f32_16x16x32_bf16 v[128:131], a[168:171], v[164:167], v[128:131]\n"
    "ds_read_b128 v[212:215], v242 offset:4096\n"
    "s_add_u32 m0, s47, 30592\n"
    "v_mfma_f32_16x16x32_bf16 v[128:131], a[192:195], v[168:171], v[128:131]\n"
    "s_add_u32 s50, s49, 0\n"
    "v_fma_f32 v106, v106, v178, v186\n"
    "v_mfma_f32_16x16x32_bf16 v[128:131], a[216:219], v[172:175], v[128:131]\n"
    "v_rcp_f32_e32 v198, v198\n"
    "v_mfma_f32_16x16x32_bf16 v[128:131], a[240:243], v[144:147], v[128:131]\n"
    "v_rcp_f32_e32 v199, v199\n"
    "v_mfma_f32_16x16x32_bf16 v[128:131], v[8:11], v[148:151], v[128:131]\n"
    "v_mul_f32_e32 v102, v102, v198\n"
    "s_waitcnt lgkmcnt(1)\n"
    "v_mfma_f32_16x16x32_bf16 v[128:131], v[32:35], v[152:155], v[128:131]\n"
    "buffer_store_dwordx4 v[208:211], v243, s[16:19], s50 offen nt\n"
    "v_mfma_f32_16x16x32_bf16 v[128:131], v[56:59], v[156:159], v[128:131]\n"
    "s_add_u32 s50, s49, 896\n"
    "v_mul_f32_e32 v103, v103, v199\n"
    "v_mfma_f32_16x16x32_bf16 v[132:135], a[172:175], v[164:167], v[132:135]\n"
    "ds_read_b128 v[164:167], v238 offset:57344\n"
    "v_cvt_pk_bf16_f32 v203, v102, v103\n"
    "v_mfma_f32_16x16x32_bf16 v[132:135], a[196:199], v[168:171], v[132:135]\n"
    "ds_read_b128 v[168:171], v235 offset:59392\n"
    "v_fma_f32 v107, v107, v179, v187\n"
    "v_mfma_f32_16x16x32_bf16 v[132:135], a[220:223], v[172:175], v[132:135]\n"
    "ds_read_b128 v[172:175], v238 offset:59392\n"
    "v_mfma_f32_16x16x32_bf16 v[132:135], a[244:247], v[144:147], v[132:135]\n"
    "ds_read_b128 v[144:147], v235 offset:61440\n"
    "v_mfma_f32_16x16x32_bf16 v[132:135], v[12:15], v[148:151], v[132:135]\n"
    "ds_read_b128 v[148:151], v238 offset:61440\n"
    "v_mfma_f32_16x16x32_bf16 v[132:135], v[36:39], v[152:155], v[132:135]\n"
    "ds_read_b128 v[152:155], v235 offset:63488\n"
    "v_mfma_f32_16x16x32_bf16 v[132:135], v[60:63], v[156:159], v[132:135]\n"
    "buffer_load_dwordx4 v216, s[8:11], s44 offen offset:128 lds\n"
    "v_mfma_f32_16x16x32_bf16 v[120:123], a[16:19], v[160:163], v[120:123]\n"
    "s_add_u32 m0, s47, 32512\n"
    "s_waitcnt lgkmcnt(4)\n"
    "v_mfma_f32_16x16x32_bf16 v[120:123], a[40:43], v[164:167], v[120:123]\n"
    "buffer_store_dwordx4 v[212:215], v243, s[16:19], s50 offen nt\n"
    "v_mfma_f32_16x16x32_bf16 v[120:123], a[64:67], v[168:171], v[120:123]\n"
    "s_waitcnt lgkmcnt(2)\n"
    "v_mfma_f32_16x16x32_bf16 v[120:123], a[88:91], v[172:175], v[120:123]\n"
    "ds_write_b128 v241, v[200:203] offset:8192\n"
    "v_mfma_f32_16x16x32_bf16 v[120:123], a[112:115], v[144:147], v[120:123]\n"
    "v_fma_f32 v108, v108, v180, v188\n"
    "s_waitcnt lgkmcnt(1)\n"
    "v_mfma_f32_16x16x32_bf16 v[120:123], a[136:139], v[148:151], v[120:123]\n"
    "v_exp_f32_e32 v192, v104\n"
    "v_mfma_f32_16x16x32_bf16 v[120:123], a[160:163], v[152:155], v[120:123]\n"
    "v_exp_f32_e32 v193, v105\n"
    "v_mfma_f32_16x16x32_bf16 v[124:127], a[20:23], v[160:163], v[124:127]\n"
    "buffer_load_dwordx4 v216, s[8:11], s44 offen offset:256 lds\n"
    "v_mfma_f32_16x16x32_bf16 v[124:127], a[44:47], v[164:167], v[124:127]\n"
    "s_add_u32 m0, s47, 34432\n"
    "v_fma_f32 v192, v192, s51, s51\n"
    "v_mfma_f32_16x16x32_bf16 v[124:127], a[68:71], v[168:171], v[124:127]\n"
    "v_fma_f32 v193, v193, s51, s51\n"
    "v_fma_f32 v109, v109, v181, v189\n"
    "v_mfma_f32_16x16x32_bf16 v[124:127], a[92:95], v[172:175], v[124:127]\n"
    "v_rcp_f32_e32 v192, v192\n"
    "v_mfma_f32_16x16x32_bf16 v[124:127], a[116:119], v[144:147], v[124:127]\n"
    "v_rcp_f32_e32 v193, v193\n"
    "v_mfma_f32_16x16x32_bf16 v[124:127], a[140:143], v[148:151], v[124:127]\n"
    "v_mul_f32_e32 v104, v104, v192\n"
    "v_mul_f32_e32 v105, v105, v193\n"
    "v_mfma_f32_16x16x32_bf16 v[124:127], a[164:167], v[152:155], v[124:127]\n"
    "v_cvt_pk_bf16_f32 v204, v104, v105\n"
    "v_fma_f32 v110, v110, v182, v190\n"
    "v_mfma_f32_16x16x32_bf16 v[128:131], a[8:11], v[160:163], v[128:131]\n"
    "v_exp_f32_e32 v194, v106\n"
    "v_mfma_f32_16x16x32_bf16 v[128:131], a[32:35], v[164:167], v[128:131]\n"
    "buffer_load_dwordx4 v216, s[8:11], s44 offen offset:384 lds\n"
    "v_mfma_f32_16x16x32_bf16 v[128:131], a[56:59], v[168:171], v[128:131]\n"
    "s_add_u32 m0, s47, 36352\n"
    "v_fma_f32 v194, v194, s51, s51\n"
    "v_mfma_f32_16x16x32_bf16 v[128:131], a[80:83], v[172:175], v[128:131]\n"
    "v_exp_f32_e32 v195, v107\n"
    "v_mfma_f32_16x16x32_bf16 v[128:131], a[104:107], v[144:147], v[128:131]\n"
    "v_fma_f32 v195, v195, s51, s51\n"
    "v_fma_f32 v111, v111, v183, v191\n"
    "v_mfma_f32_16x16x32_bf16 v[128:131], a[128:131], v[148:151], v[128:131]\n"
    "v_rcp_f32_e32 v194, v194\n"
    "v_mfma_f32_16x16x32_bf16 v[128:131], a[152:155], v[152:155], v[128:131]\n"
    "v_rcp_f32_e32 v195, v195\n"
    "v_mfma_f32_16x16x32_bf16 v[132:135], a[12:15], v[160:163], v[132:135]\n"
    "v_mul_f32_e32 v106, v106, v194\n"
    "v_mul_f32_e32 v107, v107, v195\n"
    "v_mfma_f32_16x16x32_bf16 v[132:135], a[36:39], v[164:167], v[132:135]\n"
    "v_cvt_pk_bf16_f32 v205, v106, v107\n"
    "v_mfma_f32_16x16x32_bf16 v[132:135], a[60:63], v[168:171], v[132:135]\n"
    "buffer_load_dwordx4 v216, s[8:11], s44 offen offset:512 lds\n"
    "v_mfma_f32_16x16x32_bf16 v[132:135], a[84:87], v[172:175], v[132:135]\n"
    "ds_read_b128 v[156:159], v238 offset:63488\n"
    "s_add_u32 m0, s47, 38272\n"
    "v_mfma_f32_16x16x32_bf16 v[132:135], a[108:111], v[144:147], v[132:135]\n"
    "v_exp_f32_e32 v196, v108\n"
    "v_mfma_f32_16x16x32_bf16 v[132:135], a[132:135], v[148:151], v[132:135]\n"
    "v_exp_f32_e32 v197, v109\n"
    "v_mfma_f32_16x16x32_bf16 v[132:135], a[156:159], v[152:155], v[132:135]\n"
    "v_fma_f32 v196, v196, s51, s51\n"
    "v_fma_f32 v197, v197, s51, s51\n"
    "v_mfma_f32_16x16x32_bf16 v[136:139], a[0:3], v[160:163], 0\n"
    "v_rcp_f32_e32 v196, v196\n"
    "v_mfma_f32_16x16x32_bf16 v[136:139], a[24:27], v[164:167], v[136:139]\n"
    "v_rcp_f32_e32 v197, v197\n"
    "v_mfma_f32_16x16x32_bf16 v[136:139], a[48:51], v[168:171], v[136:139]\n"
    "v_mul_f32_e32 v108, v108, v196\n"
    "v_mul_f32_e32 v109, v109, v197\n"
    "v_mfma_f32_16x16x32_bf16 v[136:139], a[72:75], v[172:175], v[136:139]\n"
    "buffer_load_dwordx4 v216, s[8:11], s44 offen offset:640 lds\n"
    "v_mfma_f32_16x16x32_bf16 v[136:139], a[96:99], v[144:147], v[136:139]\n"
    "s_add_u32 m0, s47, 40192\n"
    "v_cvt_pk_bf16_f32 v206, v108, v109\n"
    "v_mfma_f32_16x16x32_bf16 v[136:139], a[120:123], v[148:151], v[136:139]\n"
    "v_exp_f32_e32 v198, v110\n"
    "v_mfma_f32_16x16x32_bf16 v[136:139], a[144:147], v[152:155], v[136:139]\n"
    "v_exp_f32_e32 v199, v111\n"
    "v_mfma_f32_16x16x32_bf16 v[140:143], a[4:7], v[160:163], 0\n"
    "ds_read_b128 v[160:163], v236 offset:0\n"
    "v_fma_f32 v198, v198, s51, s51\n"
    "v_mfma_f32_16x16x32_bf16 v[140:143], a[28:31], v[164:167], v[140:143]\n"
    "ds_read_b128 v[164:167], v239 offset:0\n"
    "v_fma_f32 v199, v199, s51, s51\n"
    "v_mfma_f32_16x16x32_bf16 v[140:143], a[52:55], v[168:171], v[140:143]\n"
    "ds_read_b128 v[168:171], v236 offset:2048\n"
    "v_mfma_f32_16x16x32_bf16 v[140:143], a[76:79], v[172:175], v[140:143]\n"
    "ds_read_b128 v[172:175], v239 offset:2048\n"
    "v_mfma_f32_16x16x32_bf16 v[140:143], a[100:103], v[144:147], v[140:143]\n"
    "ds_read_b128 v[144:147], v236 offset:4096\n"
    "v_mfma_f32_16x16x32_bf16 v[140:143], a[124:127], v[148:151], v[140:143]\n"
    "ds_read_b128 v[148:151], v239 offset:4096\n"
    "v_mfma_f32_16x16x32_bf16 v[140:143], a[148:151], v[152:155], v[140:143]\n"
    "s_waitcnt lgkmcnt(5)\n"
    "v_mfma_f32_16x16x32_bf16 v[120:123], a[184:187], v[156:159], v[120:123]\n"
    "buffer_load_dwordx4 v216, s[8:11], s44 offen offset:768 lds\n"
    "v_mfma_f32_16x16x32_bf16 v[120:123], a[208:211], v[160:163], v[120:123]\n"
    "s_waitcnt lgkmcnt(3)\n"
    "v_mfma_f32_16x16x32_bf16 v[120:123], a[232:235], v[164:167], v[120:123]\n"
    "v_rcp_f32_e32 v198, v198\n"
    "v_mfma_f32_16x16x32_bf16 v[120:123], v[0:3], v[168:171], v[120:123]\n"
    "v_mul_f32_e32 v110, v110, v198\n"
    "s_waitcnt lgkmcnt(1)\n"
    "v_mfma_f32_16x16x32_bf16 v[120:123], v[24:27], v[172:175], v[120:123]\n"
    "v_rcp_f32_e32 v199, v199\n"
    "v_mfma_f32_16x16x32_bf16 v[120:123], v[48:51], v[144:147], v[120:123]\n"
    "v_mul_f32_e32 v111, v111, v199\n"
    "s_waitcnt lgkmcnt(0)\n"
    "v_mfma_f32_16x16x32_bf16 v[120:123], v[72:75], v[148:151], v[120:123]\n"
    "v_cvt_pk_bf16_f32 v207, v110, v111\n"
    "v_mfma_f32_16x16x32_bf16 v[124:127], a[188:191], v[156:159], v[124:127]\n"
    "ds_write_b128 v241, v[204:207] offset:12288\n"
    "v_mfma_f32_16x16x32_bf16 v[124:127], a[212:215], v[160:163], v[124:127]\n"
    "v_mfma_f32_16x16x32_bf16 v[124:127], a[236:239], v[164:167], v[124:127]\n"
    "v_mfma_f32_16x16x32_bf16 v[124:127], v[4:7], v[168:171], v[124:127]\n"
    "v_mfma_f32_16x16x32_bf16 v[124:127], v[28:31], v[172:175], v[124:127]\n"
    "v_mfma_f32_16x16x32_bf16 v[124:127], v[52:55], v[144:147], v[124:127]\n"
    "v_mfma_f32_16x16x32_bf16 v[124:127], v[76:79], v[148:151], v[124:127]\n"
    "v_mfma_f32_16x16x32_bf16 v[128:131], a[176:179], v[156:159], v[128:131]\n"
    "v_mfma_f32_16x16x32_bf16 v[128:131], a[200:203], v[160:163], v[128:131]\n"
    "v_mfma_f32_16x16x32_bf16 v[128:131], a[224:227], v[164:167], v[128:131]\n"
    "v_mfma_f32_16x16x32_bf16 v[128:131], a[248:251], v[168:171], v[128:131]\n"
    "v_mfma_f32_16x16x32_bf16 v[128:131], v[16:19], v[172:175], v[128:131]\n"
    "v_mfma_f32_16x16x32_bf16 v[128:131], v[40:43], v[144:147], v[128:131]\n"
    "v_mfma_f32_16x16x32_bf16 v[128:131], v[64:67], v[148:151], v[128:131]\n"
    "v_mfma_f32_16x16x32_bf16 v[132:135], a[180:183], v[156:159], v[132:135]\n"
    "v_mfma_f32_16x16x32_bf16 v[132:135], a[204:207], v[160:163], v[132:135]\n"
    "v_mfma_f32_16x16x32_bf16 v[132:135], a[228:231], v[164:167], v[132:135]\n"
    "v_mfma_f32_16x16x32_bf16 v[132:135], a[252:255], v[168:171], v[132:135]\n"
    "s_waitcnt vmcnt(16) lgkmcnt(0)\n"
    "s_barrier\n"
    "v_mfma_f32_16x16x32_bf16 v[132:135], v[20:23], v[172:175], v[132:135]\n"
    "ds_read_b128 v[152:155], v236 offset:20480\n"
    "s_add_u32 s44, s48, 8960\n"
    "v_mfma_f32_16x16x32_bf16 v[132:135], v[44:47], v[144:147], v[132:135]\n"
    "ds_read_b128 v[208:211], v242 offset:8192\n"
    "s_add_u32 m0, s47, 57344\n"
    "v_mfma_f32_16x16x32_bf16 v[132:135], v[68:71], v[148:151], v[132:135]\n"
    "buffer_load_dwordx4 v216, s[8:11], s44 offen offset:0 lds\n"
    "v_mfma_f32_16x16x32_bf16 v[136:139], a[168:171], v[156:159], v[136:139]\n"
    "ds_read_b128 v[212:215], v242 offset:12288\n"
    "s_add_u32 m0, s47, 59264\n"
    "v_mfma_f32_16x16x32_bf16 v[136:139], a[192:195], v[160:163], v[136:139]\n"
    "s_add_u32 s50, s49, 1792\n"
    "v_fma_f32 v112, v112, v176, v184\n"
    "v_mfma_f32_16x16x32_bf16 v[136:139], a[216:219], v[164:167], v[136:139]\n"
    "v_fma_f32 v113, v113, v177, v185\n"
    "v_fma_f32 v114, v114, v178, v186\n"
    "v_mfma_f32_16x16x32_bf16 v[136:139], a[240:243], v[168:171], v[136:139]\n"
    "v_exp_f32_e32 v192, v112\n"
    "v_mfma_f32_16x16x32_bf16 v[136:139], v[8:11], v[172:175], v[136:139]\n"
    "v_fma_f32 v192, v192, s51, s51\n"
    "s_waitcnt lgkmcnt(1)\n"
    "v_mfma_f32_16x16x32_bf16 v[136:139], v[32:35], v[144:147], v[136:139]\n"
    "buffer_store_dwordx4 v[208:211], v243, s[16:19], s50 offen nt\n"
    "v_mfma_f32_16x16x32_bf16 v[136:139], v[56:59], v[148:151], v[136:139]\n"
    "s_add_u32 s50, s49, 2688\n"
    "v_fma_f32 v115, v115, v179, v187\n"
    "v_mfma_f32_16x16x32_bf16 v[140:143], a[172:175], v[156:159], v[140:143]\n"
    "ds_read_b128 v[156:159], v239 offset:20480\n"
    "v_fma_f32 v116, v116, v180, v188\n"
    "v_mfma_f32_16x16x32_bf16 v[140:143], a[196:199], v[160:163], v[140:143]\n"
    "ds_read_b128 v[160:163], v236 offset:22528\n"
    "v_fma_f32 v117, v117, v181, v189\n"
    "v_mfma_f32_16x16x32_bf16 v[140:143], a[220:223], v[164:167], v[140:143]\n"
    "ds_read_b128 v[164:167], v239 offset:22528\n"
    "v_fma_f32 v118, v118, v182, v190\n"
    "v_mfma_f32_16x16x32_bf16 v[140:143], a[244:247], v[168:171], v[140:143]\n"
    "ds_read_b128 v[168:171], v236 offset:24576\n"
    "v_fma_f32 v119, v119, v183, v191\n"
    "v_mfma_f32_16x16x32_bf16 v[140:143], v[12:15], v[172:175], v[140:143]\n"
    "ds_read_b128 v[172:175], v239 offset:24576\n"
    "v_mfma_f32_16x16x32_bf16 v[140:143], v[36:39], v[144:147], v[140:143]\n"
    "ds_read_b128 v[144:147], v236 offset:26624\n"
    "v_mfma_f32_16x16x32_bf16 v[140:143], v[60:63], v[148:151], v[140:143]\n"
    "buffer_load_dwordx4 v216, s[8:11], s44 offen offset:128 lds\n"
    "v_mfma_f32_16x16x32_bf16 v[128:131], a[16:19], v[152:155], v[128:131]\n"
    "s_add_u32 m0, s47, 61184\n"
    "s_waitcnt lgkmcnt(4)\n"
    "v_mfma_f32_16x16x32_bf16 v[128:131], a[40:43], v[156:159], v[128:131]\n"
    "buffer_store_dwordx4 v[212:215], v243, s[16:19], s50 offen nt\n"
    "v_mfma_f32_16x16x32_bf16 v[128:131], a[64:67], v[160:163], v[128:131]\n"
    "s_waitcnt lgkmcnt(2)\n"
    "v_mfma_f32_16x16x32_bf16 v[128:131], a[88:91], v[164:167], v[128:131]\n"
    "v_exp_f32_e32 v193, v113\n"
    "v_mfma_f32_16x16x32_bf16 v[128:131], a[112:115], v[168:171], v[128:131]\n"
    "v_fma_f32 v193, v193, s51, s51\n"
    "s_waitcnt lgkmcnt(0)\n"
    "v_mfma_f32_16x16x32_bf16 v[128:131], a[136:139], v[172:175], v[128:131]\n"
    "v_rcp_f32_e32 v192, v192\n"
    "v_mfma_f32_16x16x32_bf16 v[128:131], a[160:163], v[144:147], v[128:131]\n"
    "v_rcp_f32_e32 v193, v193\n"
    "v_mfma_f32_16x16x32_bf16 v[132:135], a[20:23], v[152:155], v[132:135]\n"
    "buffer_load_dwordx4 v216, s[8:11], s44 offen offset:256 lds\n"
    "v_mfma_f32_16x16x32_bf16 v[132:135], a[44:47], v[156:159], v[132:135]\n"
    "s_add_u32 m0, s47, 63104\n"
    "v_mul_f32_e32 v112, v112, v192\n"
    "v_mfma_f32_16x16x32_bf16 v[132:135], a[68:71], v[160:163], v[132:135]\n"
    "v_mul_f32_e32 v113, v113, v193\n"
    "v_cvt_pk_bf16_f32 v200, v112, v113\n"
    "v_mfma_f32_16x16x32_bf16 v[132:135], a[92:95], v[164:167], v[132:135]\n"
    "v_exp_f32_e32 v194, v114\n"
    "v_mfma_f32_16x16x32_bf16 v[132:135], a[116:119], v[168:171], v[132:135]\n"
    "v_exp_f32_e32 v195, v115\n"
    "v_mfma_f32_16x16x32_bf16 v[132:135], a[140:143], v[172:175], v[132:135]\n"
    "v_fma_f32 v194, v194, s51, s51\n"
    "v_fma_f32 v195, v195, s51, s51\n"
    "v_mfma_f32_16x16x32_bf16 v[132:135], a[164:167], v[144:147], v[132:135]\n"
    "v_rcp_f32_e32 v194, v194\n"
    "v_mfma_f32_16x16x32_bf16 v[136:139], a[8:11], v[152:155], v[136:139]\n"
    "v_rcp_f32_e32 v195, v195\n"
    "v_mfma_f32_16x16x32_bf16 v[136:139], a[32:35], v[156:159], v[136:139]\n"
    "buffer_load_dwordx4 v216, s[8:11], s44 offen offset:384 lds\n"
    "v_mfma_f32_16x16x32_bf16 v[136:139], a[56:59], v[160:163], v[136:139]\n"
    "s_add_u32 m0, s47, 65024\n"
    "v_mul_f32_e32 v114, v114, v194\n"
    "v_mfma_f32_16x16x32_bf16 v[136:139], a[80:83], v[164:167], v[136:139]\n"
    "v_mul_f32_e32 v115, v115, v195\n"
    "v_cvt_pk_bf16_f32 v201, v114, v115\n"
    "v_mfma_f32_16x16x32_bf16 v[136:139], a[104:107], v[168:171], v[136:139]\n"
    "v_exp_f32_e32 v196, v116\n"
    "v_mfma_f32_16x16x32_bf16 v[136:139], a[128:131], v[172:175], v[136:139]\n"
    "v_exp_f32_e32 v197, v117\n"
    "v_mfma_f32_16x16x32_bf16 v[136:139], a[152:155], v[144:147], v[136:139]\n"
    "v_fma_f32 v196, v196, s51, s51\n"
    "v_fma_f32 v197, v197, s51, s51\n"
    "v_mfma_f32_16x16x32_bf16 v[140:143], a[12:15], v[152:155], v[140:143]\n"
    "v_rcp_f32_e32 v196, v196\n"
    "v_mfma_f32_16x16x32_bf16 v[140:143], a[36:39], v[156:159], v[140:143]\n"
    "v_rcp_f32_e32 v197, v197\n"
    "v_mfma_f32_16x16x32_bf16 v[140:143], a[60:63], v[160:163], v[140:143]\n"
    "buffer_load_dwordx4 v216, s[8:11], s44 offen offset:512 lds\n"
    "v_mfma_f32_16x16x32_bf16 v[140:143], a[84:87], v[164:167], v[140:143]\n"
    "ds_read_b128 v[148:151], v239 offset:26624\n"
    "s_add_u32 m0, s47, 66944\n"
    "v_mfma_f32_16x16x32_bf16 v[140:143], a[108:111], v[168:171], v[140:143]\n"
    "v_mul_f32_e32 v116, v116, v196\n"
    "v_mul_f32_e32 v117, v117, v197\n"
    "v_mfma_f32_16x16x32_bf16 v[140:143], a[132:135], v[172:175], v[140:143]\n"
    "v_cvt_pk_bf16_f32 v202, v116, v117\n"
    "v_fma_f32 v120, v120, v176, v184\n"
    "v_mfma_f32_16x16x32_bf16 v[140:143], a[156:159], v[144:147], v[140:143]\n"
    "v_exp_f32_e32 v198, v118\n"
    "v_mfma_f32_16x16x32_bf16 v[80:83], a[0:3], v[152:155], 0\n"
    "v_exp_f32_e32 v199, v119\n"
    "v_mfma_f32_16x16x32_bf16 v[80:83], a[24:27], v[156:159], v[80:83]\n"
    "v_fma_f32 v198, v198, s51, s51\n"
    "v_fma_f32 v199, v199, s51, s51\n"
    "v_mfma_f32_16x16x32_bf16 v[80:83], a[48:51], v[160:163], v[80:83]\n"
    "v_rcp_f32_e32 v198, v198\n"
    "v_mfma_f32_16x16x32_bf16 v[80:83], a[72:75], v[164:167], v[80:83]\n"
    "buffer_load_dwordx4 v216, s[8:11], s44 offen offset:640 lds\n"
    "v_mfma_f32_16x16x32_bf16 v[80:83], a[96:99], v[168:171], v[80:83]\n"
    "s_add_u32 m0, s47, 68864\n"
    "v_mul_f32_e32 v118, v118, v198\n"
    "v_mfma_f32_16x16x32_bf16 v[80:83], a[120:123], v[172:175], v[80:83]\n"
    "v_rcp_f32_e32 v199, v199\n"
    "v_mfma_f32_16x16x32_bf16 v[80:83], a[144:147], v[144:147], v[80:83]\n"
    "v_mul_f32_e32 v119, v119, v199\n"
    "v_cvt_pk_bf16_f32 v203, v118, v119\n"
    "v_mfma_f32_16x16x32_bf16 v[84:87], a[4:7], v[152:155], 0\n"
    "ds_read_b128 v[152:155], v236 offset:28672\n"
    "v_fma_f32 v121, v121, v177, v185\n"
    "v_mfma_f32_16x16x32_bf16 v[84:87], a[28:31], v[156:159], v[84:87]\n"
    "ds_read_b128 v[156:159], v239 offset:28672\n"
    "v_fma_f32 v122, v122, v178, v186\n"
    "v_mfma_f32_16x16x32_bf16 v[84:87], a[52:55], v[160:163], v[84:87]\n"
    "ds_read_b128 v[160:163], v236 offset:30720\n"
    "v_fma_f32 v123, v123, v179, v187\n"
    "v_mfma_f32_16x16x32_bf16 v[84:87], a[76:79], v[164:167], v[84:87]\n"
    "ds_read_b128 v[164:167], v239 offset:30720\n"
    "v_mfma_f32_16x16x32_bf16 v[84:87], a[100:103], v[168:171], v[84:87]\n"
    "ds_read_b128 v[168:171], v236 offset:32768\n"
    "v_mfma_f32_16x16x32_bf16 v[84:87], a[124:127], v[172:175], v[84:87]\n"
    "ds_read_b128 v[172:175], v239 offset:32768\n"
    "v_mfma_f32_16x16x32_bf16 v[84:87], a[148:151], v[144:147], v[84:87]\n"
    "s_waitcnt lgkmcnt(5)\n"
    "v_mfma_f32_16x16x32_bf16 v[128:131], a[184:187], v[148:151], v[128:131]\n"
    "buffer_load_dwordx4 v216, s[8:11], s44 offen offset:768 lds\n"
    "v_mfma_f32_16x16x32_bf16 v[128:131], a[208:211], v[152:155], v[128:131]\n"
    "s_waitcnt lgkmcnt(3)\n"
    "v_mfma_f32_16x16x32_bf16 v[128:131], a[232:235], v[156:159], v[128:131]\n"
    "ds_write_b128 v241, v[200:203] offset:0\n"
    "v_mfma_f32_16x16x32_bf16 v[128:131], v[0:3], v[160:163], v[128:131]\n"
    "v_fma_f32 v124, v124, v180, v188\n"
    "s_waitcnt lgkmcnt(2)\n"
    "v_mfma_f32_16x16x32_bf16 v[128:131], v[24:27], v[164:167], v[128:131]\n"
    "v_exp_f32_e32 v192, v120\n"
    "v_mfma_f32_16x16x32_bf16 v[128:131], v[48:51], v[168:171], v[128:131]\n"
    "v_fma_f32 v192, v192, s51, s51\n"
    "s_waitcnt lgkmcnt(1)\n"
    "v_mfma_f32_16x16x32_bf16 v[128:131], v[72:75], v[172:175], v[128:131]\n"
    "v_exp_f32_e32 v193, v121\n"
    "v_mfma_f32_16x16x32_bf16 v[132:135], a[188:191], v[148:151], v[132:135]\n"
    "v_fma_f32 v193, v193, s51, s51\n"
    "v_fma_f32 v125, v125, v181, v189\n"
    "v_mfma_f32_16x16x32_bf16 v[132:135], a[212:215], v[152:155], v[132:135]\n"
    "v_rcp_f32_e32 v192, v192\n"
    "v_mfma_f32_16x16x32_bf16 v[132:135], a[236:239], v[156:159], v[132:135]\n"
    "v_rcp_f32_e32 v193, v193\n"
    "v_mfma_f32_16x16x32_bf16 v[132:135], v[4:7], v[160:163], v[132:135]\n"
    "v_mul_f32_e32 v120, v120, v192\n"
    "v_mul_f32_e32 v121, v121, v193\n"
    "v_mfma_f32_16x16x32_bf16 v[132:135], v[28:31], v[164:167], v[132:135]\n"
    "v_cvt_pk_bf16_f32 v204, v120, v121\n"
    "v_fma_f32 v126, v126, v182, v190\n"
    "v_mfma_f32_16x16x32_bf16 v[132:135], v[52:55], v[168:171], v[132:135]\n"
    "v_exp_f32_e32 v194, v122\n"
    "v_mfma_f32_16x16x32_bf16 v[132:135], v[76:79], v[172:175], v[132:135]\n"
    "v_exp_f32_e32 v195, v123\n"
    "v_mfma_f32_16x16x32_bf16 v[136:139], a[176:179], v[148:151], v[136:139]\n"
    "v_fma_f32 v194, v194, s51, s51\n"
    "v_fma_f32 v195, v195, s51, s51\n"
    "v_mfma_f32_16x16x32_bf16 v[136:139], a[200:203], v[152:155], v[136:139]\n"
    "v_rcp_f32_e32 v194, v194\n"
    "v_mfma_f32_16x16x32_bf16 v[136:139], a[224:227], v[156:159], v[136:139]\n"
    "v_rcp_f32_e32 v195, v195\n"
    "v_mfma_f32_16x16x32_bf16 v[136:139], a[248:251], v[160:163], v[136:139]\n"
    "v_mul_f32_e32 v122, v122, v194\n"
    "v_mul_f32_e32 v123, v123, v195\n"
    "v_mfma_f32_16x16x32_bf16 v[136:139], v[16:19], v[164:167], v[136:139]\n"
    "v_cvt_pk_bf16_f32 v205, v122, v123\n"
    "v_fma_f32 v127, v127, v183, v191\n"
    "v_mfma_f32_16x16x32_bf16 v[136:139], v[40:43], v[168:171], v[136:139]\n"
    "v_exp_f32_e32 v196, v124\n"
    "v_mfma_f32_16x16x32_bf16 v[136:139], v[64:67], v[172:175], v[136:139]\n"
    "v_exp_f32_e32 v197, v125\n"
    "v_mfma_f32_16x16x32_bf16 v[140:143], a[180:183], v[148:151], v[140:143]\n"
    "v_fma_f32 v196, v196, s51, s51\n"
    "v_fma_f32 v197, v197, s51, s51\n"
    "v_mfma_f32_16x16x32_bf16 v[140:143], a[204:207], v[152:155], v[140:143]\n"
    "v_rcp_f32_e32 v196, v196\n"
    "v_mfma_f32_16x16x32_bf16 v[140:143], a[228:231], v[156:159], v[140:143]\n"
    "v_rcp_f32_e32 v197, v197\n"
    "v_mfma_f32_16x16x32_bf16 v[140:143], a[252:255], v[160:163], v[140:143]\n"
    "s_waitcnt vmcnt(18) lgkmcnt(0)\n"
    "s_barrier\n"
    "v_mfma_f32_16x16x32_bf16 v[140:143], v[20:23], v[164:167], v[140:143]\n"
    "ds_read_b128 v[144:147], v235 offset:0\n"
    "s_add_u32 s44, s48, 9856\n"
    "v_mfma_f32_16x16x32_bf16 v[140:143], v[44:47], v[168:171], v[140:143]\n"
    "ds_read_b128 v[208:211], v242 offset:0\n"
    "s_add_u32 m0, s47, 86016\n"
    "v_mfma_f32_16x16x32_bf16 v[140:143], v[68:71], v[172:175], v[140:143]\n"
    "buffer_load_dwordx4 v216, s[8:11], s44 offen offset:0 lds\n"
    "v_mfma_f32_16x16x32_bf16 v[80:83], a[168:171], v[148:151], v[80:83]\n"
    "s_add_u32 m0, s47, 87936\n"
    "v_mul_f32_e32 v124, v124, v196\n"
    "v_mfma_f32_16x16x32_bf16 v[80:83], a[192:195], v[152:155], v[80:83]\n"
    "s_add_u32 s50, s49, 3584\n"
    "v_mul_f32_e32 v125, v125, v197\n"
    "v_mfma_f32_16x16x32_bf16 v[80:83], a[216:219], v[156:159], v[80:83]\n"
    "v_cvt_pk_bf16_f32 v206, v124, v125\n"
    "v_fma_f32 v128, v128, v176, v184\n"
    "v_mfma_f32_16x16x32_bf16 v[80:83], a[240:243], v[160:163], v[80:83]\n"
    "v_exp_f32_e32 v198, v126\n"
    "v_mfma_f32_16x16x32_bf16 v[80:83], v[8:11], v[164:167], v[80:83]\n"
    "v_fma_f32 v198, v198, s51, s51\n"
    "s_waitcnt lgkmcnt(0)\n"
    "v_mfma_f32_16x16x32_bf16 v[80:83], v[32:35], v[168:171], v[80:83]\n"
    "buffer_store_dwordx4 v[208:211], v243, s[16:19], s50 offen nt\n"
    "v_mfma_f32_16x16x32_bf16 v[80:83], v[56:59], v[172:175], v[80:83]\n"
    "v_exp_f32_e32 v199, v127\n"
    "v_mfma_f32_16x16x32_bf16 v[84:87], a[172:175], v[148:151], v[84:87]\n"
    "ds_read_b128 v[148:151], v238 offset:0\n"
    "v_fma_f32 v199, v199, s51, s51\n"
    "v_mfma_f32_16x16x32_bf16 v[84:87], a[196:199], v[152:155], v[84:87]\n"
    "ds_read_b128 v[152:155], v235 offset:2048\n"
    "v_fma_f32 v129, v129, v177, v185\n"
    "v_mfma_f32_16x16x32_bf16 v[84:87], a[220:223], v[156:159], v[84:87]\n"
    "ds_read_b128 v[156:159], v238 offset:2048\n"
    "v_fma_f32 v130, v130, v178, v186\n"
    "v_mfma_f32_16x16x32_bf16 v[84:87], a[244:247], v[160:163], v[84:87]\n"
    "ds_read_b128 v[160:163], v235 offset:4096\n"
    "v_fma_f32 v131, v131, v179, v187\n"
    "v_mfma_f32_16x16x32_bf16 v[84:87], v[12:15], v[164:167], v[84:87]\n"
    "ds_read_b128 v[164:167], v238 offset:4096\n"
    "v_mfma_f32_16x16x32_bf16 v[84:87], v[36:39], v[168:171], v[84:87]\n"
    "ds_read_b128 v[168:171], v235 offset:6144\n"
    "v_mfma_f32_16x16x32_bf16 v[84:87], v[60:63], v[172:175], v[84:87]\n"
    "buffer_load_dwordx4 v216, s[8:11], s44 offen offset:128 lds\n"
    "v_mfma_f32_16x16x32_bf16 v[136:139], a[16:19], v[144:147], v[136:139]\n"
    "s_add_u32 m0, s47, 89856\n"
    "s_waitcnt lgkmcnt(4)\n"
    "v_mfma_f32_16x16x32_bf16 v[136:139], a[40:43], v[148:151], v[136:139]\n"
    "v_rcp_f32_e32 v198, v198\n"
    "v_mfma_f32_16x16x32_bf16 v[136:139], a[64:67], v[152:155], v[136:139]\n"
    "v_mul_f32_e32 v126, v126, v198\n"
    "s_waitcnt lgkmcnt(2)\n"
    "v_mfma_f32_16x16x32_bf16 v[136:139], a[88:91], v[156:159], v[136:139]\n"
    "v_rcp_f32_e32 v199, v199\n"
    "v_mfma_f32_16x16x32_bf16 v[136:139], a[112:115], v[160:163], v[136:139]\n"
    "v_mul_f32_e32 v127, v127, v199\n"
    "s_waitcnt lgkmcnt(0)\n"
    "v_mfma_f32_16x16x32_bf16 v[136:139], a[136:139], v[164:167], v[136:139]\n"
    "v_cvt_pk_bf16_f32 v207, v126, v127\n"
    "v_mfma_f32_16x16x32_bf16 v[136:139], a[160:163], v[168:171], v[136:139]\n"
    "ds_write_b128 v241, v[204:207] offset:4096\n"
    "v_mfma_f32_16x16x32_bf16 v[140:143], a[20:23], v[144:147], v[140:143]\n"
    "buffer_load_dwordx4 v216, s[8:11], s44 offen offset:256 lds\n"
    "v_mfma_f32_16x16x32_bf16 v[140:143], a[44:47], v[148:151], v[140:143]\n"
    "s_add_u32 m0, s47, 91776\n"
    "v_fma_f32 v132, v132, v180, v188\n"
    "v_mfma_f32_16x16x32_bf16 v[140:143], a[68:71], v[152:155], v[140:143]\n"
    "v_exp_f32_e32 v192, v128\n"
    "v_mfma_f32_16x16x32_bf16 v[140:143], a[92:95], v[156:159], v[140:143]\n"
    "v_exp_f32_e32 v193, v129\n"
    "v_mfma_f32_16x16x32_bf16 v[140:143], a[116:119], v[160:163], v[140:143]\n"
    "v_fma_f32 v192, v192, s51, s51\n"
    "v_fma_f32 v193, v193, s51, s51\n"
    "v_mfma_f32_16x16x32_bf16 v[140:143], a[140:143], v[164:167], v[140:143]\n"
    "v_rcp_f32_e32 v192, v192\n"
    "v_mfma_f32_16x16x32_bf16 v[140:143], a[164:167], v[168:171], v[140:143]\n"
    "v_rcp_f32_e32 v193, v193\n"
    "v_mfma_f32_16x16x32_bf16 v[80:83], a[8:11], v[144:147], v[80:83]\n"
    "v_mul_f32_e32 v128, v128, v192\n"
    "v_mul_f32_e32 v129, v129, v193\n"
    "v_mfma_f32_16x16x32_bf16 v[80:83], a[32:35], v[148:151], v[80:83]\n"
    "buffer_load_dwordx4 v216, s[8:11], s44 offen offset:384 lds\n"
    "v_mfma_f32_16x16x32_bf16 v[80:83], a[56:59], v[152:155], v[80:83]\n"
    "s_add_u32 m0, s47, 93696\n"
    "v_cvt_pk_bf16_f32 v200, v128, v129\n"
    "v_mfma_f32_16x16x32_bf16 v[80:83], a[80:83], v[156:159], v[80:83]\n"
    "v_exp_f32_e32 v194, v130\n"
    "v_mfma_f32_16x16x32_bf16 v[80:83], a[104:107], v[160:163], v[80:83]\n"
    "v_exp_f32_e32 v195, v131\n"
    "v_mfma_f32_16x16x32_bf16 v[80:83], a[128:131], v[164:167], v[80:83]\n"
    "v_fma_f32 v194, v194, s51, s51\n"
    "v_fma_f32 v195, v195, s51, s51\n"
    "v_mfma_f32_16x16x32_bf16 v[80:83], a[152:155], v[168:171], v[80:83]\n"
    "v_rcp_f32_e32 v194, v194\n"
    "v_mfma_f32_16x16x32_bf16 v[84:87], a[12:15], v[144:147], v[84:87]\n"
    "v_rcp_f32_e32 v195, v195\n"
    "v_mfma_f32_16x16x32_bf16 v[84:87], a[36:39], v[148:151], v[84:87]\n"
    "v_mul_f32_e32 v130, v130, v194\n"
    "v_mul_f32_e32 v131, v131, v195\n"
    "v_mfma_f32_16x16x32_bf16 v[84:87], a[60:63], v[152:155], v[84:87]\n"
    "buffer_load_dwordx4 v216, s[8:11], s44 offen offset:512 lds\n"
    "v_mfma_f32_16x16x32_bf16 v[84:87], a[84:87], v[156:159], v[84:87]\n"
    "ds_read_b128 v[172:175], v238 offset:6144\n"
    "s_add_u32 m0, s47, 95616\n"
    "v_mfma_f32_16x16x32_bf16 v[84:87], a[108:111], v[160:163], v[84:87]\n"
    "v_cvt_pk_bf16_f32 v201, v130, v131\n"
    "v_fma_f32 v133, v133, v181, v189\n"
    "v_mfma_f32_16x16x32_bf16 v[84:87], a[132:135], v[164:167], v[84:87]\n"
    "v_exp_f32_e32 v196, v132\n"
    "v_mfma_f32_16x16x32_bf16 v[84:87], a[156:159], v[168:171], v[84:87]\n"
    "v_exp_f32_e32 v197, v133\n"
    "v_mfma_f32_16x16x32_bf16 v[88:91], a[0:3], v[144:147], 0\n"
    "v_fma_f32 v196, v196, s51, s51\n"
    "v_fma_f32 v197, v197, s51, s51\n"
    "v_mfma_f32_16x16x32_bf16 v[88:91], a[24:27], v[148:151], v[88:91]\n"
    "v_rcp_f32_e32 v196, v196\n"
    "v_mfma_f32_16x16x32_bf16 v[88:91], a[48:51], v[152:155], v[88:91]\n"
    "v_rcp_f32_e32 v197, v197\n"
    "v_mfma_f32_16x16x32_bf16 v[88:91], a[72:75], v[156:159], v[88:91]\n"
    "buffer_load_dwordx4 v216, s[8:11], s44 offen offset:640 lds\n"
    "v_mfma_f32_16x16x32_bf16 v[88:91], a[96:99], v[160:163], v[88:91]\n"
    "s_add_u32 m0, s47, 97536\n"
    "v_mul_f32_e32 v132, v132, v196\n"
    "v_mfma_f32_16x16x32_bf16 v[88:91], a[120:123], v[164:167], v[88:91]\n"
    "v_mul_f32_e32 v133, v133, v197\n"
    "v_cvt_pk_bf16_f32 v202, v132, v133\n"
    "v_mfma_f32_16x16x32_bf16 v[88:91], a[144:147], v[168:171], v[88:91]\n"
    "v_fma_f32 v134, v134, v182, v190\n"
    "v_fma_f32 v135, v135, v183, v191\n"
    "v_mfma_f32_16x16x32_bf16 v[92:95], a[4:7], v[144:147], 0\n"
    "ds_read_b128 v[144:147], v235 offset:8192\n"
    "v_mfma_f32_16x16x32_bf16 v[92:95], a[28:31], v[148:151], v[92:95]\n"
    "ds_read_b128 v[148:151], v238 offset:8192\n"
    "v_mfma_f32_16x16x32_bf16 v[92:95], a[52:55], v[152:155], v[92:95]\n"
    "ds_read_b128 v[152:155], v235 offset:10240\n"
    "v_mfma_f32_16x16x32_bf16 v[92:95], a[76:79], v[156:159], v[92:95]\n"
    "ds_read_b128 v[156:159], v238 offset:10240\n"
    "v_mfma_f32_16x16x32_bf16 v[92:95], a[100:103], v[160:163], v[92:95]\n"
    "ds_read_b128 v[160:163], v235 offset:12288\n"
    "v_mfma_f32_16x16x32_bf16 v[92:95], a[124:127], v[164:167], v[92:95]\n"
    "ds_read_b128 v[164:167], v238 offset:12288\n"
    "v_mfma_f32_16x16x32_bf16 v[92:95], a[148:151], v[168:171], v[92:95]\n"
    "s_waitcnt lgkmcnt(5)\n"
    "v_mfma_f32_16x16x32_bf16 v[136:139], a[184:187], v[172:175], v[136:139]\n"
    "buffer_load_dwordx4 v216, s[8:11], s44 offen offset:768 lds\n"
    "v_mfma_f32_16x16x32_bf16 v[136:139], a[208:211], v[144:147], v[136:139]\n"
    "s_waitcnt lgkmcnt(3)\n"
    "v_mfma_f32_16x16x32_bf16 v[136:139], a[232:235], v[148:151], v[136:139]\n"
    "v_exp_f32_e32 v198, v134\n"
    "v_mfma_f32_16x16x32_bf16 v[136:139], v[0:3], v[152:155], v[136:139]\n"
    "v_fma_f32 v198, v198, s51, s51\n"
    "s_waitcnt lgkmcnt(1)\n"
    "v_mfma_f32_16x16x32_bf16 v[136:139], v[24:27], v[156:159], v[136:139]\n"
    "v_exp_f32_e32 v199, v135\n"
    "v_mfma_f32_16x16x32_bf16 v[136:139], v[48:51], v[160:163], v[136:139]\n"
    "v_fma_f32 v199, v199, s51, s51\n"
    "s_waitcnt lgkmcnt(0)\n"
    "v_mfma_f32_16x16x32_bf16 v[136:139], v[72:75], v[164:167], v[136:139]\n"
    "v_rcp_f32_e32 v198, v198\n"
    "v_mfma_f32_16x16x32_bf16 v[140:143], a[188:191], v[172:175], v[140:143]\n"
    "v_rcp_f32_e32 v199, v199\n"
    "v_mfma_f32_16x16x32_bf16 v[140:143], a[212:215], v[144:147], v[140:143]\n"
    "v_mul_f32_e32 v134, v134, v198\n"
    "v_mul_f32_e32 v135, v135, v199\n"
    "v_mfma_f32_16x16x32_bf16 v[140:143], a[236:239], v[148:151], v[140:143]\n"
    "v_cvt_pk_bf16_f32 v203, v134, v135\n"
    "v_mfma_f32_16x16x32_bf16 v[140:143], v[4:7], v[152:155], v[140:143]\n"
    "ds_write_b128 v241, v[200:203] offset:8192\n"
    "v_mfma_f32_16x16x32_bf16 v[140:143], v[28:31], v[156:159], v[140:143]\n"
    "v_mfma_f32_16x16x32_bf16 v[140:143], v[52:55], v[160:163], v[140:143]\n"
    "v_mfma_f32_16x16x32_bf16 v[140:143], v[76:79], v[164:167], v[140:143]\n"
    "v_mfma_f32_16x16x32_bf16 v[80:83], a[176:179], v[172:175], v[80:83]\n"
    "v_fma_f32 v136, v136, v176, v184\n"
    "v_fma_f32 v137, v137, v177, v185\n"
    "v_mfma_f32_16x16x32_bf16 v[80:83], a[200:203], v[144:147], v[80:83]\n"
    "v_exp_f32_e32 v192, v136\n"
    "v_mfma_f32_16x16x32_bf16 v[80:83], a[224:227], v[148:151], v[80:83]\n"
    "v_exp_f32_e32 v193, v137\n"
    "v_mfma_f32_16x16x32_bf16 v[80:83], a[248:251], v[152:155], v[80:83]\n"
    "v_fma_f32 v192, v192, s51, s51\n"
    "v_fma_f32 v193, v193, s51, s51\n"
    "v_mfma_f32_16x16x32_bf16 v[80:83], v[16:19], v[156:159], v[80:83]\n"
    "v_rcp_f32_e32 v192, v192\n"
    "v_mfma_f32_16x16x32_bf16 v[80:83], v[40:43], v[160:163], v[80:83]\n"
    "v_rcp_f32_e32 v193, v193\n"
    "v_mfma_f32_16x16x32_bf16 v[80:83], v[64:67], v[164:167], v[80:83]\n"
    "v_mul_f32_e32 v136, v136, v192\n"
    "v_mul_f32_e32 v137, v137, v193\n"
    "v_mfma_f32_16x16x32_bf16 v[84:87], a[180:183], v[172:175], v[84:87]\n"
    "v_cvt_pk_bf16_f32 v204, v136, v137\n"
    "v_fma_f32 v138, v138, v178, v186\n"
    "v_mfma_f32_16x16x32_bf16 v[84:87], a[204:207], v[144:147], v[84:87]\n"
    "v_fma_f32 v139, v139, v179, v187\n"
    "v_mfma_f32_16x16x32_bf16 v[84:87], a[228:231], v[148:151], v[84:87]\n"
    "v_exp_f32_e32 v194, v138\n"
    "v_mfma_f32_16x16x32_bf16 v[84:87], a[252:255], v[152:155], v[84:87]\n"
    "s_waitcnt vmcnt(17) lgkmcnt(0)\n"
    "s_barrier\n"
    "v_mfma_f32_16x16x32_bf16 v[84:87], v[20:23], v[156:159], v[84:87]\n"
    "ds_read_b128 v[168:171], v235 offset:28672\n"
    "s_add_u32 s44, s48, 10752\n"
    "v_mfma_f32_16x16x32_bf16 v[84:87], v[44:47], v[160:163], v[84:87]\n"
    "ds_read_b128 v[212:215], v242 offset:4096\n"
    "s_add_u32 m0, s47, 0\n"
    "v_mfma_f32_16x16x32_bf16 v[84:87], v[68:71], v[164:167], v[84:87]\n"
    "buffer_load_dwordx4 v216, s[8:11], s44 offen offset:0 lds\n"
    "v_mfma_f32_16x16x32_bf16 v[88:91], a[168:171], v[172:175], v[88:91]\n"
    "ds_read_b128 v[208:211], v242 offset:8192\n"
    "s_add_u32 m0, s47, 1920\n"
    "v_mfma_f32_16x16x32_bf16 v[88:91], a[192:195], v[144:147], v[88:91]\n"
    "s_add_u32 s50, s49, 4480\n"
    "v_fma_f32 v194, v194, s51, s51\n"
    "v_mfma_f32_16x16x32_bf16 v[88:91], a[216:219], v[148:151], v[88:91]\n"
    "v_exp_f32_e32 v195, v139\n"
    "v_mfma_f32_16x16x32_bf16 v[88:91], a[240:243], v[152:155], v[88:91]\n"
    "v_fma_f32 v195, v195, s51, s51\n"
    "v_fma_f32 v140, v140, v180, v188\n"
    "v_mfma_f32_16x16x32_bf16 v[88:91], v[8:11], v[156:159], v[88:91]\n"
    "v_fma_f32 v141, v141, v181, v189\n"
    "s_waitcnt lgkmcnt(1)\n"
    "v_mfma_f32_16x16x32_bf16 v[88:91], v[32:35], v[160:163], v[88:91]\n"
    "buffer_store_dwordx4 v[212:215], v243, s[16:19], s50 offen nt\n"
    "v_mfma_f32_16x16x32_bf16 v[88:91], v[56:59], v[164:167], v[88:91]\n"
    "s_add_u32 s50, s49, 5376\n"
    "v_fma_f32 v142, v142, v182, v190\n"
    "v_mfma_f32_16x16x32_bf16 v[92:95], a[172:175], v[172:175], v[92:95]\n"
    "ds_read_b128 v[172:175], v238 offset:28672\n"
    "v_fma_f32 v143, v143, v183, v191\n"
    "v_mfma_f32_16x16x32_bf16 v[92:95], a[196:199], v[144:147], v[92:95]\n"
    "ds_read_b128 v[144:147], v235 offset:30720\n"
    "v_mfma_f32_16x16x32_bf16 v[92:95], a[220:223], v[148:151], v[92:95]\n"
    "ds_read_b128 v[148:151], v238 offset:30720\n"
    "v_mfma_f32_16x16x32_bf16 v[92:95], a[244:247], v[152:155], v[92:95]\n"
    "ds_read_b128 v[152:155], v235 offset:32768\n"
    "v_mfma_f32_16x16x32_bf16 v[92:95], v[12:15], v[156:159], v[92:95]\n"
    "ds_read_b128 v[156:159], v238 offset:32768\n"
    "v_mfma_f32_16x16x32_bf16 v[92:95], v[36:39], v[160:163], v[92:95]\n"
    "ds_read_b128 v[160:163], v235 offset:34816\n"
    "v_mfma_f32_16x16x32_bf16 v[92:95], v[60:63], v[164:167], v[92:95]\n"
    "buffer_load_dwordx4 v216, s[8:11], s44 offen offset:128 lds\n"
    "v_mfma_f32_16x16x32_bf16 v[80:83], a[16:19], v[168:171], v[80:83]\n"
    "s_add_u32 m0, s47, 3840\n"
    "s_waitcnt lgkmcnt(4)\n"
    "v_mfma_f32_16x16x32_bf16 v[80:83], a[40:43], v[172:175], v[80:83]\n"
    "buffer_store_dwordx4 v[208:211], v243, s[16:19], s50 offen nt\n"
    "v_mfma_f32_16x16x32_bf16 v[80:83], a[64:67], v[144:147], v[80:83]\n"
    "s_waitcnt lgkmcnt(2)\n"
    "v_mfma_f32_16x16x32_bf16 v[80:83], a[88:91], v[148:151], v[80:83]\n"
    "v_rcp_f32_e32 v194, v194\n"
    "v_mfma_f32_16x16x32_bf16 v[80:83], a[112:115], v[152:155], v[80:83]\n"
    "v_mul_f32_e32 v138, v138, v194\n"
    "s_waitcnt lgkmcnt(0)\n"
    "v_mfma_f32_16x16x32_bf16 v[80:83], a[136:139], v[156:159], v[80:83]\n"
    "v_rcp_f32_e32 v195, v195\n"
    "v_mfma_f32_16x16x32_bf16 v[80:83], a[160:163], v[160:163], v[80:83]\n"
    "v_mul_f32_e32 v139, v139, v195\n"
    "v_cvt_pk_bf16_f32 v205, v138, v139\n"
    "v_mfma_f32_16x16x32_bf16 v[84:87], a[20:23], v[168:171], v[84:87]\n"
    "buffer_load_dwordx4 v216, s[8:11], s44 offen offset:256 lds\n"
    "v_mfma_f32_16x16x32_bf16 v[84:87], a[44:47], v[172:175], v[84:87]\n"
    "s_add_u32 m0, s47, 5760\n"
    "v_mfma_f32_16x16x32_bf16 v[84:87], a[68:71], v[144:147], v[84:87]\n"
    "v_exp_f32_e32 v196, v140\n"
    "v_mfma_f32_16x16x32_bf16 v[84:87], a[92:95], v[148:151], v[84:87]\n"
    "v_exp_f32_e32 v197, v141\n"
    "v_mfma_f32_16x16x32_bf16 v[84:87], a[116:119], v[152:155], v[84:87]\n"
    "v_fma_f32 v196, v196, s51, s51\n"
    "v_fma_f32 v197, v197, s51, s51\n"
    "v_mfma_f32_16x16x32_bf16 v[84:87], a[140:143], v[156:159], v[84:87]\n"
    "v_rcp_f32_e32 v196, v196\n"
    "v_mfma_f32_16x16x32_bf16 v[84:87], a[164:167], v[160:163], v[84:87]\n"
    "v_rcp_f32_e32 v197, v197\n"
    "v_mfma_f32_16x16x32_bf16 v[88:91], a[8:11], v[168:171], v[88:91]\n"
    "v_mul_f32_e32 v140, v140, v196\n"
    "v_mul_f32_e32 v141, v141, v197\n"
    "v_mfma_f32_16x16x32_bf16 v[88:91], a[32:35], v[172:175], v[88:91]\n"
    "buffer_load_dwordx4 v216, s[8:11], s44 offen offset:384 lds\n"
    "v_mfma_f32_16x16x32_bf16 v[88:91], a[56:59], v[144:147], v[88:91]\n"
    "s_add_u32 m0, s47, 7680\n"
    "v_cvt_pk_bf16_f32 v206, v140, v141\n"
    "v_mfma_f32_16x16x32_bf16 v[88:91], a[80:83], v[148:151], v[88:91]\n"
    "v_exp_f32_e32 v198, v142\n"
    "v_mfma_f32_16x16x32_bf16 v[88:91], a[104:107], v[152:155], v[88:91]\n"
    "v_exp_f32_e32 v199, v143\n"
    "v_mfma_f32_16x16x32_bf16 v[88:91], a[128:131], v[156:159], v[88:91]\n"
    "v_fma_f32 v198, v198, s51, s51\n"
    "v_fma_f32 v199, v199, s51, s51\n"
    "v_mfma_f32_16x16x32_bf16 v[88:91], a[152:155], v[160:163], v[88:91]\n"
    "v_rcp_f32_e32 v198, v198\n"
    "v_mfma_f32_16x16x32_bf16 v[92:95], a[12:15], v[168:171], v[92:95]\n"
    "v_rcp_f32_e32 v199, v199\n"
    "v_mfma_f32_16x16x32_bf16 v[92:95], a[36:39], v[172:175], v[92:95]\n"
    "v_mul_f32_e32 v142, v142, v198\n"
    "v_mul_f32_e32 v143, v143, v199\n"
    "v_mfma_f32_16x16x32_bf16 v[92:95], a[60:63], v[144:147], v[92:95]\n"
    "buffer_load_dwordx4 v216, s[8:11], s44 offen offset:512 lds\n"
    "v_mfma_f32_16x16x32_bf16 v[92:95], a[84:87], v[148:151], v[92:95]\n"
    "ds_read_b128 v[164:167], v238 offset:34816\n"
    "s_add_u32 m0, s47, 9600\n"
    "v_mfma_f32_16x16x32_bf16 v[92:95], a[108:111], v[152:155], v[92:95]\n"
    "v_cvt_pk_bf16_f32 v207, v142, v143\n"
    "v_mfma_f32_16x16x32_bf16 v[92:95], a[132:135], v[156:159], v[92:95]\n"
    "ds_write_b128 v241, v[204:207] offset:12288\n"
    "v_mfma_f32_16x16x32_bf16 v[92:95], a[156:159], v[160:163], v[92:95]\n"
    "v_mfma_f32_16x16x32_bf16 v[96:99], a[0:3], v[168:171], 0\n"
    "v_mfma_f32_16x16x32_bf16 v[96:99], a[24:27], v[172:175], v[96:99]\n"
    "v_mfma_f32_16x16x32_bf16 v[96:99], a[48:51], v[144:147], v[96:99]\n"
    "v_mfma_f32_16x16x32_bf16 v[96:99], a[72:75], v[148:151], v[96:99]\n"
    "buffer_load_dwordx4 v216, s[8:11], s44 offen offset:640 lds\n"
    "v_mfma_f32_16x16x32_bf16 v[96:99], a[96:99], v[152:155], v[96:99]\n"
    "s_add_u32 m0, s47, 11520\n"
    "v_mfma_f32_16x16x32_bf16 v[96:99], a[120:123], v[156:159], v[96:99]\n"
    "v_mfma_f32_16x16x32_bf16 v[96:99], a[144:147], v[160:163], v[96:99]\n"
    "v_mfma_f32_16x16x32_bf16 v[100:103], a[4:7], v[168:171], 0\n"
    "ds_read_b128 v[168:171], v235 offset:36864\n"
    "v_mfma_f32_16x16x32_bf16 v[100:103], a[28:31], v[172:175], v[100:103]\n"
    "ds_read_b128 v[172:175], v238 offset:36864\n"
    "v_mfma_f32_16x16x32_bf16 v[100:103], a[52:55], v[144:147], v[100:103]\n"
    "ds_read_b128 v[144:147], v235 offset:38912\n"
    "v_mfma_f32_16x16x32_bf16 v[100:103], a[76:79], v[148:151], v[100:103]\n"
    "ds_read_b128 v[148:151], v238 offset:38912\n"
    "v_mfma_f32_16x16x32_bf16 v[100:103], a[100:103], v[152:155], v[100:103]\n"
    "ds_read_b128 v[152:155], v235 offset:40960\n"
    "v_mfma_f32_16x16x32_bf16 v[100:103], a[124:127], v[156:159], v[100:103]\n"
    "ds_read_b128 v[156:159], v238 offset:40960\n"
    "v_mfma_f32_16x16x32_bf16 v[100:103], a[148:151], v[160:163], v[100:103]\n"
    "s_waitcnt lgkmcnt(5)\n"
    "v_mfma_f32_16x16x32_bf16 v[80:83], a[184:187], v[164:167], v[80:83]\n"
    "buffer_load_dwordx4 v216, s[8:11], s44 offen offset:768 lds\n"
    "v_mfma_f32_16x16x32_bf16 v[80:83], a[208:211], v[168:171], v[80:83]\n"
    "s_waitcnt lgkmcnt(3)\n"
    "v_mfma_f32_16x16x32_bf16 v[80:83], a[232:235], v[172:175], v[80:83]\n"
    "v_mfma_f32_16x16x32_bf16 v[80:83], v[0:3], v[144:147], v[80:83]\n"
    "s_waitcnt lgkmcnt(1)\n"
    "v_mfma_f32_16x16x32_bf16 v[80:83], v[24:27], v[148:151], v[80:83]\n"
    "v_mfma_f32_16x16x32_bf16 v[80:83], v[48:51], v[152:155], v[80:83]\n"
    "s_waitcnt lgkmcnt(0)\n"
    "v_mfma_f32_16x16x32_bf16 v[80:83], v[72:75], v[156:159], v[80:83]\n"
    "v_mfma_f32_16x16x32_bf16 v[84:87], a[188:191], v[164:167], v[84:87]\n"
    "v_mfma_f32_16x16x32_bf16 v[84:87], a[212:215], v[168:171], v[84:87]\n"
    "v_mfma_f32_16x16x32_bf16 v[84:87], a[236:239], v[172:175], v[84:87]\n"
    "v_mfma_f32_16x16x32_bf16 v[84:87], v[4:7], v[144:147], v[84:87]\n"
    "v_mfma_f32_16x16x32_bf16 v[84:87], v[28:31], v[148:151], v[84:87]\n"
    "v_mfma_f32_16x16x32_bf16 v[84:87], v[52:55], v[152:155], v[84:87]\n"
    "v_mfma_f32_16x16x32_bf16 v[84:87], v[76:79], v[156:159], v[84:87]\n"
    "v_mfma_f32_16x16x32_bf16 v[88:91], a[176:179], v[164:167], v[88:91]\n"
    "v_fma_f32 v80, v80, v176, v184\n"
    "v_fma_f32 v81, v81, v177, v185\n"
    "v_mfma_f32_16x16x32_bf16 v[88:91], a[200:203], v[168:171], v[88:91]\n"
    "v_exp_f32_e32 v192, v80\n"
    "v_mfma_f32_16x16x32_bf16 v[88:91], a[224:227], v[172:175], v[88:91]\n"
    "v_exp_f32_e32 v193, v81\n"
    "v_mfma_f32_16x16x32_bf16 v[88:91], a[248:251], v[144:147], v[88:91]\n"
    "v_fma_f32 v192, v192, s51, s51\n"
    "v_fma_f32 v193, v193, s51, s51\n"
    "v_mfma_f32_16x16x32_bf16 v[88:91], v[16:19], v[148:151], v[88:91]\n"
    "v_rcp_f32_e32 v192, v192\n"
    "v_mfma_f32_16x16x32_bf16 v[88:91], v[40:43], v[152:155], v[88:91]\n"
    "v_rcp_f32_e32 v193, v193\n"
    "v_mfma_f32_16x16x32_bf16 v[88:91], v[64:67], v[156:159], v[88:91]\n"
    "v_mul_f32_e32 v80, v80, v192\n"
    "v_mul_f32_e32 v81, v81, v193\n"
    "v_mfma_f32_16x16x32_bf16 v[92:95], a[180:183], v[164:167], v[92:95]\n"
    "v_cvt_pk_bf16_f32 v200, v80, v81\n"
    "v_fma_f32 v82, v82, v178, v186\n"
    "v_mfma_f32_16x16x32_bf16 v[92:95], a[204:207], v[168:171], v[92:95]\n"
    "v_fma_f32 v83, v83, v179, v187\n"
    "v_mfma_f32_16x16x32_bf16 v[92:95], a[228:231], v[172:175], v[92:95]\n"
    "v_exp_f32_e32 v194, v82\n"
    "v_mfma_f32_16x16x32_bf16 v[92:95], a[252:255], v[144:147], v[92:95]\n"
    "s_waitcnt vmcnt(17)\n"
    "s_barrier\n"
    "v_mfma_f32_16x16x32_bf16 v[92:95], v[20:23], v[148:151], v[92:95]\n"
    "ds_read_b128 v[160:163], v235 offset:57344\n"
    "s_add_u32 s44, s48, 11648\n"
    "v_mfma_f32_16x16x32_bf16 v[92:95], v[44:47], v[152:155], v[92:95]\n"
    "ds_read_b128 v[212:215], v242 offset:12288\n"
    "s_add_u32 m0, s47, 28672\n"
    "v_mfma_f32_16x16x32_bf16 v[92:95], v[68:71], v[156:159], v[92:95]\n"
    "buffer_load_dwordx4 v216, s[8:11], s44 offen offset:0 lds\n"
    "v_mfma_f32_16x16x32_bf16 v[96:99], a[168:171], v[164:167], v[96:99]\n"
    "s_add_u32 m0, s47, 30592\n"
    "v_fma_f32 v194, v194, s51, s51\n"
    "v_mfma_f32_16x16x32_bf16 v[96:99], a[192:195], v[168:171], v[96:99]\n"
    "s_add_u32 s50, s49, 6272\n"
    "v_fma_f32 v84, v84, v180, v188\n"
    "v_mfma_f32_16x16x32_bf16 v[96:99], a[216:219], v[172:175], v[96:99]\n"
    "v_exp_f32_e32 v195, v83\n"
    "v_mfma_f32_16x16x32_bf16 v[96:99], a[240:243], v[144:147], v[96:99]\n"
    "v_fma_f32 v195, v195, s51, s51\n"
    "v_fma_f32 v85, v85, v181, v189\n"
    "v_mfma_f32_16x16x32_bf16 v[96:99], v[8:11], v[148:151], v[96:99]\n"
    "v_fma_f32 v86, v86, v182, v190\n"
    "s_waitcnt lgkmcnt(0)\n"
    "v_mfma_f32_16x16x32_bf16 v[96:99], v[32:35], v[152:155], v[96:99]\n"
    "buffer_store_dwordx4 v[212:215], v243, s[16:19], s50 offen nt\n"
    "v_mfma_f32_16x16x32_bf16 v[96:99], v[56:59], v[156:159], v[96:99]\n"
    "v_rcp_f32_e32 v194, v194\n"
    "v_mfma_f32_16x16x32_bf16 v[100:103], a[172:175], v[164:167], v[100:103]\n"
    "ds_read_b128 v[164:167], v238 offset:57344\n"
    "v_mul_f32_e32 v82, v82, v194\n"
    "v_mfma_f32_16x16x32_bf16 v[100:103], a[196:199], v[168:171], v[100:103]\n"
    "ds_read_b128 v[168:171], v235 offset:59392\n"
    "v_fma_f32 v87, v87, v183, v191\n"
    "v_mfma_f32_16x16x32_bf16 v[100:103], a[220:223], v[172:175], v[100:103]\n"
    "ds_read_b128 v[172:175], v238 offset:59392\n"
    "v_mfma_f32_16x16x32_bf16 v[100:103], a[244:247], v[144:147], v[100:103]\n"
    "ds_read_b128 v[144:147], v235 offset:61440\n"
    "v_mfma_f32_16x16x32_bf16 v[100:103], v[12:15], v[148:151], v[100:103]\n"
    "ds_read_b128 v[148:151], v238 offset:61440\n"
    "v_mfma_f32_16x16x32_bf16 v[100:103], v[36:39], v[152:155], v[100:103]\n"
    "ds_read_b128 v[152:155], v235 offset:63488\n"
    "v_mfma_f32_16x16x32_bf16 v[100:103], v[60:63], v[156:159], v[100:103]\n"
    "buffer_load_dwordx4 v216, s[8:11], s44 offen offset:128 lds\n"
    "v_mfma_f32_16x16x32_bf16 v[88:91], a[16:19], v[160:163], v[88:91]\n"
    "s_add_u32 m0, s47, 32512\n"
    "s_waitcnt lgkmcnt(4)\n"
    "v_mfma_f32_16x16x32_bf16 v[88:91], a[40:43], v[164:167], v[88:91]\n"
    "v_rcp_f32_e32 v195, v195\n"
    "v_mfma_f32_16x16x32_bf16 v[88:91], a[64:67], v[168:171], v[88:91]\n"
    "v_mul_f32_e32 v83, v83, v195\n"
    "s_waitcnt lgkmcnt(2)\n"
    "v_mfma_f32_16x16x32_bf16 v[88:91], a[88:91], v[172:175], v[88:91]\n"
    "v_cvt_pk_bf16_f32 v201, v82, v83\n"
    "v_mfma_f32_16x16x32_bf16 v[88:91], a[112:115], v[144:147], v[88:91]\n"
    "s_waitcnt lgkmcnt(0)\n"
    "v_mfma_f32_16x16x32_bf16 v[88:91], a[136:139], v[148:151], v[88:91]\n"
    "v_exp_f32_e32 v196, v84\n"
    "v_mfma_f32_16x16x32_bf16 v[88:91], a[160:163], v[152:155], v[88:91]\n"
    "v_exp_f32_e32 v197, v85\n"
    "v_mfma_f32_16x16x32_bf16 v[92:95], a[20:23], v[160:163], v[92:95]\n"
    "buffer_load_dwordx4 v216, s[8:11], s44 offen offset:256 lds\n"
    "v_mfma_f32_16x16x32_bf16 v[92:95], a[44:47], v[164:167], v[92:95]\n"
    "s_add_u32 m0, s47, 34432\n"
    "v_fma_f32 v196, v196, s51, s51\n"
    "v_mfma_f32_16x16x32_bf16 v[92:95], a[68:71], v[168:171], v[92:95]\n"
    "v_fma_f32 v197, v197, s51, s51\n"
    "v_mfma_f32_16x16x32_bf16 v[92:95], a[92:95], v[172:175], v[92:95]\n"
    "v_rcp_f32_e32 v196, v196\n"
    "v_mfma_f32_16x16x32_bf16 v[92:95], a[116:119], v[144:147], v[92:95]\n"
    "v_rcp_f32_e32 v197, v197\n"
    "v_mfma_f32_16x16x32_bf16 v[92:95], a[140:143], v[148:151], v[92:95]\n"
    "v_mul_f32_e32 v84, v84, v196\n"
    "v_mul_f32_e32 v85, v85, v197\n"
    "v_mfma_f32_16x16x32_bf16 v[92:95], a[164:167], v[152:155], v[92:95]\n"
    "v_cvt_pk_bf16_f32 v202, v84, v85\n"
    "v_mfma_f32_16x16x32_bf16 v[96:99], a[8:11], v[160:163], v[96:99]\n"
    "v_exp_f32_e32 v198, v86\n"
    "v_mfma_f32_16x16x32_bf16 v[96:99], a[32:35], v[164:167], v[96:99]\n"
    "buffer_load_dwordx4 v216, s[8:11], s44 offen offset:384 lds\n"
    "v_mfma_f32_16x16x32_bf16 v[96:99], a[56:59], v[168:171], v[96:99]\n"
    "s_add_u32 m0, s47, 36352\n"
    "v_fma_f32 v198, v198, s51, s51\n"
    "v_mfma_f32_16x16x32_bf16 v[96:99], a[80:83], v[172:175], v[96:99]\n"
    "v_exp_f32_e32 v199, v87\n"
    "v_mfma_f32_16x16x32_bf16 v[96:99], a[104:107], v[144:147], v[96:99]\n"
    "v_fma_f32 v199, v199, s51, s51\n"
    "v_mfma_f32_16x16x32_bf16 v[96:99], a[128:131], v[148:151], v[96:99]\n"
    "v_rcp_f32_e32 v198, v198\n"
    "v_mfma_f32_16x16x32_bf16 v[96:99], a[152:155], v[152:155], v[96:99]\n"
    "v_rcp_f32_e32 v199, v199\n"
    "v_mfma_f32_16x16x32_bf16 v[100:103], a[12:15], v[160:163], v[100:103]\n"
    "v_mul_f32_e32 v86, v86, v198\n"
    "v_mul_f32_e32 v87, v87, v199\n"
    "v_mfma_f32_16x16x32_bf16 v[100:103], a[36:39], v[164:167], v[100:103]\n"
    "v_cvt_pk_bf16_f32 v203, v86, v87\n"
    "v_mfma_f32_16x16x32_bf16 v[100:103], a[60:63], v[168:171], v[100:103]\n"
    "buffer_load_dwordx4 v216, s[8:11], s44 offen offset:512 lds\n"
    "v_mfma_f32_16x16x32_bf16 v[100:103], a[84:87], v[172:175], v[100:103]\n"
    "ds_read_b128 v[156:159], v238 offset:63488\n"
    "s_add_u32 m0, s47, 38272\n"
    "v_mfma_f32_16x16x32_bf16 v[100:103], a[108:111], v[144:147], v[100:103]\n"
    "ds_write_b128 v241, v[200:203] offset:0\n"
    "v_mfma_f32_16x16x32_bf16 v[100:103], a[132:135], v[148:151], v[100:103]\n"
    "v_mfma_f32_16x16x32_bf16 v[100:103], a[156:159], v[152:155], v[100:103]\n"
    "v_mfma_f32_16x16x32_bf16 v[104:107], a[0:3], v[160:163], 0\n"
    "v_mfma_f32_16x16x32_bf16 v[104:107], a[24:27], v[164:167], v[104:107]\n"
    "v_mfma_f32_16x16x32_bf16 v[104:107], a[48:51], v[168:171], v[104:107]\n"
    "v_mfma_f32_16x16x32_bf16 v[104:107], a[72:75], v[172:175], v[104:107]\n"
    "buffer_load_dwordx4 v216, s[8:11], s44 offen offset:640 lds\n"
    "v_mfma_f32_16x16x32_bf16 v[104:107], a[96:99], v[144:147], v[104:107]\n"
    "s_add_u32 m0, s47, 40192\n"
    "v_mfma_f32_16x16x32_bf16 v[104:107], a[120:123], v[148:151], v[104:107]\n"
    "v_mfma_f32_16x16x32_bf16 v[104:107], a[144:147], v[152:155], v[104:107]\n"
    "v_mfma_f32_16x16x32_bf16 v[108:111], a[4:7], v[160:163], 0\n"
    "ds_read_b128 v[160:163], v236 offset:0\n"
    "v_mfma_f32_16x16x32_bf16 v[108:111], a[28:31], v[164:167], v[108:111]\n"
    "ds_read_b128 v[164:167], v239 offset:0\n"
    "v_mfma_f32_16x16x32_bf16 v[108:111], a[52:55], v[168:171], v[108:111]\n"
    "ds_read_b128 v[168:171], v236 offset:2048\n"
    "v_mfma_f32_16x16x32_bf16 v[108:111], a[76:79], v[172:175], v[108:111]\n"
    "ds_read_b128 v[172:175], v239 offset:2048\n"
    "v_mfma_f32_16x16x32_bf16 v[108:111], a[100:103], v[144:147], v[108:111]\n"
    "ds_read_b128 v[144:147], v236 offset:4096\n"
    "v_mfma_f32_16x16x32_bf16 v[108:111], a[124:127], v[148:151], v[108:111]\n"
    "ds_read_b128 v[148:151], v239 offset:4096\n"
    "v_mfma_f32_16x16x32_bf16 v[108:111], a[148:151], v[152:155], v[108:111]\n"
    "s_waitcnt lgkmcnt(5)\n"
    "v_mfma_f32_16x16x32_bf16 v[88:91], a[184:187], v[156:159], v[88:91]\n"
    "buffer_load_dwordx4 v216, s[8:11], s44 offen offset:768 lds\n"
    "v_mfma_f32_16x16x32_bf16 v[88:91], a[208:211], v[160:163], v[88:91]\n"
    "s_waitcnt lgkmcnt(3)\n"
    "v_mfma_f32_16x16x32_bf16 v[88:91], a[232:235], v[164:167], v[88:91]\n"
    "v_mfma_f32_16x16x32_bf16 v[88:91], v[0:3], v[168:171], v[88:91]\n"
    "s_waitcnt lgkmcnt(1)\n"
    "v_mfma_f32_16x16x32_bf16 v[88:91], v[24:27], v[172:175], v[88:91]\n"
    "v_mfma_f32_16x16x32_bf16 v[88:91], v[48:51], v[144:147], v[88:91]\n"
    "s_waitcnt lgkmcnt(0)\n"
    "v_mfma_f32_16x16x32_bf16 v[88:91], v[72:75], v[148:151], v[88:91]\n"
    "v_mfma_f32_16x16x32_bf16 v[92:95], a[188:191], v[156:159], v[92:95]\n"
    "v_mfma_f32_16x16x32_bf16 v[92:95], a[212:215], v[160:163], v[92:95]\n"
    "v_mfma_f32_16x16x32_bf16 v[92:95], a[236:239], v[164:167], v[92:95]\n"
    "v_mfma_f32_16x16x32_bf16 v[92:95], v[4:7], v[168:171], v[92:95]\n"
    "v_mfma_f32_16x16x32_bf16 v[92:95], v[28:31], v[172:175], v[92:95]\n"
    "v_mfma_f32_16x16x32_bf16 v[92:95], v[52:55], v[144:147], v[92:95]\n"
    "v_mfma_f32_16x16x32_bf16 v[92:95], v[76:79], v[148:151], v[92:95]\n"
    "v_mfma_f32_16x16x32_bf16 v[96:99], a[176:179], v[156:159], v[96:99]\n"
    "v_fma_f32 v88, v88, v176, v184\n"
    "v_fma_f32 v89, v89, v177, v185\n"
    "v_mfma_f32_16x16x32_bf16 v[96:99], a[200:203], v[160:163], v[96:99]\n"
    "v_exp_f32_e32 v192, v88\n"
    "v_mfma_f32_16x16x32_bf16 v[96:99], a[224:227], v[164:167], v[96:99]\n"
    "v_exp_f32_e32 v193, v89\n"
    "v_mfma_f32_16x16x32_bf16 v[96:99], a[248:251], v[168:171], v[96:99]\n"
    "v_fma_f32 v192, v192, s51, s51\n"
    "v_fma_f32 v193, v193, s51, s51\n"
    "v_mfma_f32_16x16x32_bf16 v[96:99], v[16:19], v[172:175], v[96:99]\n"
    "v_rcp_f32_e32 v192, v192\n"
    "v_mfma_f32_16x16x32_bf16 v[96:99], v[40:43], v[144:147], v[96:99]\n"
    "v_rcp_f32_e32 v193, v193\n"
    "v_mfma_f32_16x16x32_bf16 v[96:99], v[64:67], v[148:151], v[96:99]\n"
    "v_mul_f32_e32 v88, v88, v192\n"
    "v_mul_f32_e32 v89, v89, v193\n"
    "v_mfma_f32_16x16x32_bf16 v[100:103], a[180:183], v[156:159], v[100:103]\n"
    "v_cvt_pk_bf16_f32 v204, v88, v89\n"
    "v_fma_f32 v90, v90, v178, v186\n"
    "v_mfma_f32_16x16x32_bf16 v[100:103], a[204:207], v[160:163], v[100:103]\n"
    "v_fma_f32 v91, v91, v179, v187\n"
    "v_mfma_f32_16x16x32_bf16 v[100:103], a[228:231], v[164:167], v[100:103]\n"
    "v_exp_f32_e32 v194, v90\n"
    "v_mfma_f32_16x16x32_bf16 v[100:103], a[252:255], v[168:171], v[100:103]\n"
    "s_waitcnt vmcnt(17)\n"
    "s_barrier\n"
    "v_mfma_f32_16x16x32_bf16 v[100:103], v[20:23], v[172:175], v[100:103]\n"
    "ds_read_b128 v[152:155], v236 offset:20480\n"
    "s_add_u32 s44, s48, 12544\n"
    "v_mfma_f32_16x16x32_bf16 v[100:103], v[44:47], v[144:147], v[100:103]\n"
    "ds_read_b128 v[208:211], v242 offset:0\n"
    "s_add_u32 m0, s47, 57344\n"
    "v_mfma_f32_16x16x32_bf16 v[100:103], v[68:71], v[148:151], v[100:103]\n"
    "buffer_load_dwordx4 v216, s[8:11], s44 offen offset:0 lds\n"
    "v_mfma_f32_16x16x32_bf16 v[104:107], a[168:171], v[156:159], v[104:107]\n"
    "s_add_u32 m0, s47, 59264\n"
    "v_fma_f32 v194, v194, s51, s51\n"
    "v_mfma_f32_16x16x32_bf16 v[104:107], a[192:195], v[160:163], v[104:107]\n"
    "s_add_u32 s50, s49, 7168\n"
    "v_fma_f32 v92, v92, v180, v188\n"
    "v_mfma_f32_16x16x32_bf16 v[104:107], a[216:219], v[164:167], v[104:107]\n"
    "v_exp_f32_e32 v195, v91\n"
    "v_mfma_f32_16x16x32_bf16 v[104:107], a[240:243], v[168:171], v[104:107]\n"
    "v_fma_f32 v195, v195, s51, s51\n"
    "v_fma_f32 v93, v93, v181, v189\n"
    "v_mfma_f32_16x16x32_bf16 v[104:107], v[8:11], v[172:175], v[104:107]\n"
    "v_fma_f32 v94, v94, v182, v190\n"
    "s_waitcnt lgkmcnt(0)\n"
    "v_mfma_f32_16x16x32_bf16 v[104:107], v[32:35], v[144:147], v[104:107]\n"
    "buffer_store_dwordx4 v[208:211], v243, s[16:19], s50 offen nt\n"
    "v_mfma_f32_16x16x32_bf16 v[104:107], v[56:59], v[148:151], v[104:107]\n"
    "v_rcp_f32_e32 v194, v194\n"
    "v_mfma_f32_16x16x32_bf16 v[108:111], a[172:175], v[156:159], v[108:111]\n"
    "ds_read_b128 v[156:159], v239 offset:20480\n"
    "v_mul_f32_e32 v90, v90, v194\n"
    "v_mfma_f32_16x16x32_bf16 v[108:111], a[196:199], v[160:163], v[108:111]\n"
    "ds_read_b128 v[160:163], v236 offset:22528\n"
    "v_fma_f32 v95, v95, v183, v191\n"
    "v_mfma_f32_16x16x32_bf16 v[108:111], a[220:223], v[164:167], v[108:111]\n"
    "ds_read_b128 v[164:167], v239 offset:22528\n"
    "v_mfma_f32_16x16x32_bf16 v[108:111], a[244:247], v[168:171], v[108:111]\n"
    "ds_read_b128 v[168:171], v236 offset:24576\n"
    "v_mfma_f32_16x16x32_bf16 v[108:111], v[12:15], v[172:175], v[108:111]\n"
    "ds_read_b128 v[172:175], v239 offset:24576\n"
    "v_mfma_f32_16x16x32_bf16 v[108:111], v[36:39], v[144:147], v[108:111]\n"
    "ds_read_b128 v[144:147], v236 offset:26624\n"
    "v_mfma_f32_16x16x32_bf16 v[108:111], v[60:63], v[148:151], v[108:111]\n"
    "buffer_load_dwordx4 v216, s[8:11], s44 offen offset:128 lds\n"
    "v_mfma_f32_16x16x32_bf16 v[96:99], a[16:19], v[152:155], v[96:99]\n"
    "s_add_u32 m0, s47, 61184\n"
    "s_waitcnt lgkmcnt(4)\n"
    "v_mfma_f32_16x16x32_bf16 v[96:99], a[40:43], v[156:159], v[96:99]\n"
    "v_rcp_f32_e32 v195, v195\n"
    "v_mfma_f32_16x16x32_bf16 v[96:99], a[64:67], v[160:163], v[96:99]\n"
    "v_mul_f32_e32 v91, v91, v195\n"
    "s_waitcnt lgkmcnt(2)\n"
    "v_mfma_f32_16x16x32_bf16 v[96:99], a[88:91], v[164:167], v[96:99]\n"
    "v_cvt_pk_bf16_f32 v205, v90, v91\n"
    "v_mfma_f32_16x16x32_bf16 v[96:99], a[112:115], v[168:171], v[96:99]\n"
    "s_waitcnt lgkmcnt(0)\n"
    "v_mfma_f32_16x16x32_bf16 v[96:99], a[136:139], v[172:175], v[96:99]\n"
    "v_exp_f32_e32 v196, v92\n"
    "v_mfma_f32_16x16x32_bf16 v[96:99], a[160:163], v[144:147], v[96:99]\n"
    "v_exp_f32_e32 v197, v93\n"
    "v_mfma_f32_16x16x32_bf16 v[100:103], a[20:23], v[152:155], v[100:103]\n"
    "buffer_load_dwordx4 v216, s[8:11], s44 offen offset:256 lds\n"
    "v_mfma_f32_16x16x32_bf16 v[100:103], a[44:47], v[156:159], v[100:103]\n"
    "s_add_u32 m0, s47, 63104\n"
    "v_fma_f32 v196, v196, s51, s51\n"
    "v_mfma_f32_16x16x32_bf16 v[100:103], a[68:71], v[160:163], v[100:103]\n"
    "v_fma_f32 v197, v197, s51, s51\n"
    "v_mfma_f32_16x16x32_bf16 v[100:103], a[92:95], v[164:167], v[100:103]\n"
    "v_rcp_f32_e32 v196, v196\n"
    "v_mfma_f32_16x16x32_bf16 v[100:103], a[116:119], v[168:171], v[100:103]\n"
    "v_rcp_f32_e32 v197, v197\n"
    "v_mfma_f32_16x16x32_bf16 v[100:103], a[140:143], v[172:175], v[100:103]\n"
    "v_mul_f32_e32 v92, v92, v196\n"
    "v_mul_f32_e32 v93, v93, v197\n"
    "v_mfma_f32_16x16x32_bf16 v[100:103], a[164:167], v[144:147], v[100:103]\n"
    "v_cvt_pk_bf16_f32 v206, v92, v93\n"
    "v_mfma_f32_16x16x32_bf16 v[104:107], a[8:11], v[152:155], v[104:107]\n"
    "v_exp_f32_e32 v198, v94\n"
    "v_mfma_f32_16x16x32_bf16 v[104:107], a[32:35], v[156:159], v[104:107]\n"
    "buffer_load_dwordx4 v216, s[8:11], s44 offen offset:384 lds\n"
    "v_mfma_f32_16x16x32_bf16 v[104:107], a[56:59], v[160:163], v[104:107]\n"
    "s_add_u32 m0, s47, 65024\n"
    "v_fma_f32 v198, v198, s51, s51\n"
    "v_mfma_f32_16x16x32_bf16 v[104:107], a[80:83], v[164:167], v[104:107]\n"
    "v_exp_f32_e32 v199, v95\n"
    "v_mfma_f32_16x16x32_bf16 v[104:107], a[104:107], v[168:171], v[104:107]\n"
    "v_fma_f32 v199, v199, s51, s51\n"
    "v_mfma_f32_16x16x32_bf16 v[104:107], a[128:131], v[172:175], v[104:107]\n"
    "v_rcp_f32_e32 v198, v198\n"
    "v_mfma_f32_16x16x32_bf16 v[104:107], a[152:155], v[144:147], v[104:107]\n"
    "v_rcp_f32_e32 v199, v199\n"
    "v_mfma_f32_16x16x32_bf16 v[108:111], a[12:15], v[152:155], v[108:111]\n"
    "v_mul_f32_e32 v94, v94, v198\n"
    "v_mul_f32_e32 v95, v95, v199\n"
    "v_mfma_f32_16x16x32_bf16 v[108:111], a[36:39], v[156:159], v[108:111]\n"
    "v_cvt_pk_bf16_f32 v207, v94, v95\n"
    "v_mfma_f32_16x16x32_bf16 v[108:111], a[60:63], v[160:163], v[108:111]\n"
    "buffer_load_dwordx4 v216, s[8:11], s44 offen offset:512 lds\n"
    "v_mfma_f32_16x16x32_bf16 v[108:111], a[84:87], v[164:167], v[108:111]\n"
    "ds_read_b128 v[148:151], v239 offset:26624\n"
    "s_add_u32 m0, s47, 66944\n"
    "v_mfma_f32_16x16x32_bf16 v[108:111], a[108:111], v[168:171], v[108:111]\n"
    "ds_write_b128 v241, v[204:207] offset:4096\n"
    "v_mfma_f32_16x16x32_bf16 v[108:111], a[132:135], v[172:175], v[108:111]\n"
    "v_mfma_f32_16x16x32_bf16 v[108:111], a[156:159], v[144:147], v[108:111]\n"
    "v_mfma_f32_16x16x32_bf16 v[112:115], a[0:3], v[152:155], 0\n"
    "v_mfma_f32_16x16x32_bf16 v[112:115], a[24:27], v[156:159], v[112:115]\n"
    "v_mfma_f32_16x16x32_bf16 v[112:115], a[48:51], v[160:163], v[112:115]\n"
    "v_mfma_f32_16x16x32_bf16 v[112:115], a[72:75], v[164:167], v[112:115]\n"
    "buffer_load_dwordx4 v216, s[8:11], s44 offen offset:640 lds\n"
    "v_mfma_f32_16x16x32_bf16 v[112:115], a[96:99], v[168:171], v[112:115]\n"
    "s_add_u32 m0, s47, 68864\n"
    "v_mfma_f32_16x16x32_bf16 v[112:115], a[120:123], v[172:175], v[112:115]\n"
    "v_mfma_f32_16x16x32_bf16 v[112:115], a[144:147], v[144:147], v[112:115]\n"
    "v_mfma_f32_16x16x32_bf16 v[116:119], a[4:7], v[152:155], 0\n"
    "ds_read_b128 v[152:155], v236 offset:28672\n"
    "v_mfma_f32_16x16x32_bf16 v[116:119], a[28:31], v[156:159], v[116:119]\n"
    "ds_read_b128 v[156:159], v239 offset:28672\n"
    "v_mfma_f32_16x16x32_bf16 v[116:119], a[52:55], v[160:163], v[116:119]\n"
    "ds_read_b128 v[160:163], v236 offset:30720\n"
    "v_mfma_f32_16x16x32_bf16 v[116:119], a[76:79], v[164:167], v[116:119]\n"
    "ds_read_b128 v[164:167], v239 offset:30720\n"
    "v_mfma_f32_16x16x32_bf16 v[116:119], a[100:103], v[168:171], v[116:119]\n"
    "ds_read_b128 v[168:171], v236 offset:32768\n"
    "v_mfma_f32_16x16x32_bf16 v[116:119], a[124:127], v[172:175], v[116:119]\n"
    "ds_read_b128 v[172:175], v239 offset:32768\n"
    "v_mfma_f32_16x16x32_bf16 v[116:119], a[148:151], v[144:147], v[116:119]\n"
    "s_waitcnt lgkmcnt(5)\n"
    "v_mfma_f32_16x16x32_bf16 v[96:99], a[184:187], v[148:151], v[96:99]\n"
    "buffer_load_dwordx4 v216, s[8:11], s44 offen offset:768 lds\n"
    "v_mfma_f32_16x16x32_bf16 v[96:99], a[208:211], v[152:155], v[96:99]\n"
    "s_waitcnt lgkmcnt(3)\n"
    "v_mfma_f32_16x16x32_bf16 v[96:99], a[232:235], v[156:159], v[96:99]\n"
    "v_mfma_f32_16x16x32_bf16 v[96:99], v[0:3], v[160:163], v[96:99]\n"
    "s_waitcnt lgkmcnt(1)\n"
    "v_mfma_f32_16x16x32_bf16 v[96:99], v[24:27], v[164:167], v[96:99]\n"
    "v_mfma_f32_16x16x32_bf16 v[96:99], v[48:51], v[168:171], v[96:99]\n"
    "s_waitcnt lgkmcnt(0)\n"
    "v_mfma_f32_16x16x32_bf16 v[96:99], v[72:75], v[172:175], v[96:99]\n"
    "v_mfma_f32_16x16x32_bf16 v[100:103], a[188:191], v[148:151], v[100:103]\n"
    "v_mfma_f32_16x16x32_bf16 v[100:103], a[212:215], v[152:155], v[100:103]\n"
    "v_mfma_f32_16x16x32_bf16 v[100:103], a[236:239], v[156:159], v[100:103]\n"
    "v_mfma_f32_16x16x32_bf16 v[100:103], v[4:7], v[160:163], v[100:103]\n"
    "v_mfma_f32_16x16x32_bf16 v[100:103], v[28:31], v[164:167], v[100:103]\n"
    "v_mfma_f32_16x16x32_bf16 v[100:103], v[52:55], v[168:171], v[100:103]\n"
    "v_mfma_f32_16x16x32_bf16 v[100:103], v[76:79], v[172:175], v[100:103]\n"
    "v_mfma_f32_16x16x32_bf16 v[104:107], a[176:179], v[148:151], v[104:107]\n"
    "v_fma_f32 v96, v96, v176, v184\n"
    "v_fma_f32 v97, v97, v177, v185\n"
    "v_mfma_f32_16x16x32_bf16 v[104:107], a[200:203], v[152:155], v[104:107]\n"
    "v_exp_f32_e32 v192, v96\n"
    "v_mfma_f32_16x16x32_bf16 v[104:107], a[224:227], v[156:159], v[104:107]\n"
    "v_exp_f32_e32 v193, v97\n"
    "v_mfma_f32_16x16x32_bf16 v[104:107], a[248:251], v[160:163], v[104:107]\n"
    "v_fma_f32 v192, v192, s51, s51\n"
    "v_fma_f32 v193, v193, s51, s51\n"
    "v_mfma_f32_16x16x32_bf16 v[104:107], v[16:19], v[164:167], v[104:107]\n"
    "v_rcp_f32_e32 v192, v192\n"
    "v_mfma_f32_16x16x32_bf16 v[104:107], v[40:43], v[168:171], v[104:107]\n"
    "v_rcp_f32_e32 v193, v193\n"
    "v_mfma_f32_16x16x32_bf16 v[104:107], v[64:67], v[172:175], v[104:107]\n"
    "v_mul_f32_e32 v96, v96, v192\n"
    "v_mul_f32_e32 v97, v97, v193\n"
    "v_mfma_f32_16x16x32_bf16 v[108:111], a[180:183], v[148:151], v[108:111]\n"
    "v_cvt_pk_bf16_f32 v200, v96, v97\n"
    "v_fma_f32 v98, v98, v178, v186\n"
    "v_mfma_f32_16x16x32_bf16 v[108:111], a[204:207], v[152:155], v[108:111]\n"
    "v_fma_f32 v99, v99, v179, v187\n"
    "v_mfma_f32_16x16x32_bf16 v[108:111], a[228:231], v[156:159], v[108:111]\n"
    "v_exp_f32_e32 v194, v98\n"
    "v_mfma_f32_16x16x32_bf16 v[108:111], a[252:255], v[160:163], v[108:111]\n"
    "s_waitcnt vmcnt(16)\n"
    "s_barrier\n"
    "v_mfma_f32_16x16x32_bf16 v[108:111], v[20:23], v[164:167], v[108:111]\n"
    "ds_read_b128 v[144:147], v235 offset:0\n"
    "s_add_u32 s44, s48, 13440\n"
    "v_mfma_f32_16x16x32_bf16 v[108:111], v[44:47], v[168:171], v[108:111]\n"
    "ds_read_b128 v[212:215], v242 offset:4096\n"
    "s_add_u32 m0, s47, 86016\n"
    "v_mfma_f32_16x16x32_bf16 v[108:111], v[68:71], v[172:175], v[108:111]\n"
    "buffer_load_dwordx4 v216, s[8:11], s44 offen offset:0 lds\n"
    "v_mfma_f32_16x16x32_bf16 v[112:115], a[168:171], v[148:151], v[112:115]\n"
    "s_add_u32 m0, s47, 87936\n"
    "v_fma_f32 v194, v194, s51, s51\n"
    "v_mfma_f32_16x16x32_bf16 v[112:115], a[192:195], v[152:155], v[112:115]\n"
    "s_add_u32 s50, s49, 8064\n"
    "v_fma_f32 v100, v100, v180, v188\n"
    "v_mfma_f32_16x16x32_bf16 v[112:115], a[216:219], v[156:159], v[112:115]\n"
    "v_exp_f32_e32 v195, v99\n"
    "v_mfma_f32_16x16x32_bf16 v[112:115], a[240:243], v[160:163], v[112:115]\n"
    "v_fma_f32 v195, v195, s51, s51\n"
    "v_fma_f32 v101, v101, v181, v189\n"
    "v_mfma_f32_16x16x32_bf16 v[112:115], v[8:11], v[164:167], v[112:115]\n"
    "v_fma_f32 v102, v102, v182, v190\n"
    "s_waitcnt lgkmcnt(0)\n"
    "v_mfma_f32_16x16x32_bf16 v[112:115], v[32:35], v[168:171], v[112:115]\n"
    "buffer_store_dwordx4 v[212:215], v243, s[16:19], s50 offen nt\n"
    "v_mfma_f32_16x16x32_bf16 v[112:115], v[56:59], v[172:175], v[112:115]\n"
    "v_rcp_f32_e32 v194, v194\n"
    "v_mfma_f32_16x16x32_bf16 v[116:119], a[172:175], v[148:151], v[116:119]\n"
    "ds_read_b128 v[148:151], v238 offset:0\n"
    "v_mul_f32_e32 v98, v98, v194\n"
    "v_mfma_f32_16x16x32_bf16 v[116:119], a[196:199], v[152:155], v[116:119]\n"
    "ds_read_b128 v[152:155], v235 offset:2048\n"
    "v_fma_f32 v103, v103, v183, v191\n"
    "v_mfma_f32_16x16x32_bf16 v[116:119], a[220:223], v[156:159], v[116:119]\n"
    "ds_read_b128 v[156:159], v238 offset:2048\n"
    "v_mfma_f32_16x16x32_bf16 v[116:119], a[244:247], v[160:163], v[116:119]\n"
    "ds_read_b128 v[160:163], v235 offset:4096\n"
    "v_mfma_f32_16x16x32_bf16 v[116:119], v[12:15], v[164:167], v[116:119]\n"
    "ds_read_b128 v[164:167], v238 offset:4096\n"
    "v_mfma_f32_16x16x32_bf16 v[116:119], v[36:39], v[168:171], v[116:119]\n"
    "ds_read_b128 v[168:171], v235 offset:6144\n"
    "v_mfma_f32_16x16x32_bf16 v[116:119], v[60:63], v[172:175], v[116:119]\n"
    "buffer_load_dwordx4 v216, s[8:11], s44 offen offset:128 lds\n"
    "v_mfma_f32_16x16x32_bf16 v[104:107], a[16:19], v[144:147], v[104:107]\n"
    "s_add_u32 m0, s47, 89856\n"
    "s_waitcnt lgkmcnt(4)\n"
    "v_mfma_f32_16x16x32_bf16 v[104:107], a[40:43], v[148:151], v[104:107]\n"
    "v_rcp_f32_e32 v195, v195\n"
    "v_mfma_f32_16x16x32_bf16 v[104:107], a[64:67], v[152:155], v[104:107]\n"
    "v_mul_f32_e32 v99, v99, v195\n"
    "s_waitcnt lgkmcnt(2)\n"
    "v_mfma_f32_16x16x32_bf16 v[104:107], a[88:91], v[156:159], v[104:107]\n"
    "v_cvt_pk_bf16_f32 v201, v98, v99\n"
    "v_mfma_f32_16x16x32_bf16 v[104:107], a[112:115], v[160:163], v[104:107]\n"
    "s_waitcnt lgkmcnt(0)\n"
    "v_mfma_f32_16x16x32_bf16 v[104:107], a[136:139], v[164:167], v[104:107]\n"
    "v_exp_f32_e32 v196, v100\n"
    "v_mfma_f32_16x16x32_bf16 v[104:107], a[160:163], v[168:171], v[104:107]\n"
    "v_exp_f32_e32 v197, v101\n"
    "v_mfma_f32_16x16x32_bf16 v[108:111], a[20:23], v[144:147], v[108:111]\n"
    "buffer_load_dwordx4 v216, s[8:11], s44 offen offset:256 lds\n"
    "v_mfma_f32_16x16x32_bf16 v[108:111], a[44:47], v[148:151], v[108:111]\n"
    "s_add_u32 m0, s47, 91776\n"
    "v_fma_f32 v196, v196, s51, s51\n"
    "v_mfma_f32_16x16x32_bf16 v[108:111], a[68:71], v[152:155], v[108:111]\n"
    "v_fma_f32 v197, v197, s51, s51\n"
    "v_mfma_f32_16x16x32_bf16 v[108:111], a[92:95], v[156:159], v[108:111]\n"
    "v_rcp_f32_e32 v196, v196\n"
    "v_mfma_f32_16x16x32_bf16 v[108:111], a[116:119], v[160:163], v[108:111]\n"
    "v_rcp_f32_e32 v197, v197\n"
    "v_mfma_f32_16x16x32_bf16 v[108:111], a[140:143], v[164:167], v[108:111]\n"
    "v_mul_f32_e32 v100, v100, v196\n"
    "v_mul_f32_e32 v101, v101, v197\n"
    "v_mfma_f32_16x16x32_bf16 v[108:111], a[164:167], v[168:171], v[108:111]\n"
    "v_cvt_pk_bf16_f32 v202, v100, v101\n"
    "v_mfma_f32_16x16x32_bf16 v[112:115], a[8:11], v[144:147], v[112:115]\n"
    "v_exp_f32_e32 v198, v102\n"
    "v_mfma_f32_16x16x32_bf16 v[112:115], a[32:35], v[148:151], v[112:115]\n"
    "buffer_load_dwordx4 v216, s[8:11], s44 offen offset:384 lds\n"
    "v_mfma_f32_16x16x32_bf16 v[112:115], a[56:59], v[152:155], v[112:115]\n"
    "s_add_u32 m0, s47, 93696\n"
    "v_fma_f32 v198, v198, s51, s51\n"
    "v_mfma_f32_16x16x32_bf16 v[112:115], a[80:83], v[156:159], v[112:115]\n"
    "v_exp_f32_e32 v199, v103\n"
    "v_mfma_f32_16x16x32_bf16 v[112:115], a[104:107], v[160:163], v[112:115]\n"
    "v_fma_f32 v199, v199, s51, s51\n"
    "v_mfma_f32_16x16x32_bf16 v[112:115], a[128:131], v[164:167], v[112:115]\n"
    "v_rcp_f32_e32 v198, v198\n"
    "v_mfma_f32_16x16x32_bf16 v[112:115], a[152:155], v[168:171], v[112:115]\n"
    "v_rcp_f32_e32 v199, v199\n"
    "v_mfma_f32_16x16x32_bf16 v[116:119], a[12:15], v[144:147], v[116:119]\n"
    "v_mul_f32_e32 v102, v102, v198\n"
    "v_mul_f32_e32 v103, v103, v199\n"
    "v_mfma_f32_16x16x32_bf16 v[116:119], a[36:39], v[148:151], v[116:119]\n"
    "v_cvt_pk_bf16_f32 v203, v102, v103\n"
    "v_mfma_f32_16x16x32_bf16 v[116:119], a[60:63], v[152:155], v[116:119]\n"
    "buffer_load_dwordx4 v216, s[8:11], s44 offen offset:512 lds\n"
    "v_mfma_f32_16x16x32_bf16 v[116:119], a[84:87], v[156:159], v[116:119]\n"
    "ds_read_b128 v[172:175], v238 offset:6144\n"
    "s_add_u32 m0, s47, 95616\n"
    "v_mfma_f32_16x16x32_bf16 v[116:119], a[108:111], v[160:163], v[116:119]\n"
    "ds_write_b128 v241, v[200:203] offset:8192\n"
    "v_mfma_f32_16x16x32_bf16 v[116:119], a[132:135], v[164:167], v[116:119]\n"
    "v_mfma_f32_16x16x32_bf16 v[116:119], a[156:159], v[168:171], v[116:119]\n"
    "v_mfma_f32_16x16x32_bf16 v[120:123], a[0:3], v[144:147], 0\n"
    "v_mfma_f32_16x16x32_bf16 v[120:123], a[24:27], v[148:151], v[120:123]\n"
    "v_mfma_f32_16x16x32_bf16 v[120:123], a[48:51], v[152:155], v[120:123]\n"
    "v_mfma_f32_16x16x32_bf16 v[120:123], a[72:75], v[156:159], v[120:123]\n"
    "buffer_load_dwordx4 v216, s[8:11], s44 offen offset:640 lds\n"
    "v_mfma_f32_16x16x32_bf16 v[120:123], a[96:99], v[160:163], v[120:123]\n"
    "s_add_u32 m0, s47, 97536\n"
    "v_mfma_f32_16x16x32_bf16 v[120:123], a[120:123], v[164:167], v[120:123]\n"
    "v_mfma_f32_16x16x32_bf16 v[120:123], a[144:147], v[168:171], v[120:123]\n"
    "v_mfma_f32_16x16x32_bf16 v[124:127], a[4:7], v[144:147], 0\n"
    "ds_read_b128 v[144:147], v235 offset:8192\n"
    "v_mfma_f32_16x16x32_bf16 v[124:127], a[28:31], v[148:151], v[124:127]\n"
    "ds_read_b128 v[148:151], v238 offset:8192\n"
    "v_mfma_f32_16x16x32_bf16 v[124:127], a[52:55], v[152:155], v[124:127]\n"
    "ds_read_b128 v[152:155], v235 offset:10240\n"
    "v_mfma_f32_16x16x32_bf16 v[124:127], a[76:79], v[156:159], v[124:127]\n"
    "ds_read_b128 v[156:159], v238 offset:10240\n"
    "v_mfma_f32_16x16x32_bf16 v[124:127], a[100:103], v[160:163], v[124:127]\n"
    "ds_read_b128 v[160:163], v235 offset:12288\n"
    "v_mfma_f32_16x16x32_bf16 v[124:127], a[124:127], v[164:167], v[124:127]\n"
    "ds_read_b128 v[164:167], v238 offset:12288\n"
    "v_mfma_f32_16x16x32_bf16 v[124:127], a[148:151], v[168:171], v[124:127]\n"
    "s_waitcnt lgkmcnt(5)\n"
    "v_mfma_f32_16x16x32_bf16 v[104:107], a[184:187], v[172:175], v[104:107]\n"
    "buffer_load_dwordx4 v216, s[8:11], s44 offen offset:768 lds\n"
    "v_mfma_f32_16x16x32_bf16 v[104:107], a[208:211], v[144:147], v[104:107]\n"
    "s_waitcnt lgkmcnt(3)\n"
    "v_mfma_f32_16x16x32_bf16 v[104:107], a[232:235], v[148:151], v[104:107]\n"
    "v_mfma_f32_16x16x32_bf16 v[104:107], v[0:3], v[152:155], v[104:107]\n"
    "s_waitcnt lgkmcnt(1)\n"
    "v_mfma_f32_16x16x32_bf16 v[104:107], v[24:27], v[156:159], v[104:107]\n"
    "v_mfma_f32_16x16x32_bf16 v[104:107], v[48:51], v[160:163], v[104:107]\n"
    "s_waitcnt lgkmcnt(0)\n"
    "v_mfma_f32_16x16x32_bf16 v[104:107], v[72:75], v[164:167], v[104:107]\n"
    "v_mfma_f32_16x16x32_bf16 v[108:111], a[188:191], v[172:175], v[108:111]\n"
    "v_mfma_f32_16x16x32_bf16 v[108:111], a[212:215], v[144:147], v[108:111]\n"
    "v_mfma_f32_16x16x32_bf16 v[108:111], a[236:239], v[148:151], v[108:111]\n"
    "v_mfma_f32_16x16x32_bf16 v[108:111], v[4:7], v[152:155], v[108:111]\n"
    "v_mfma_f32_16x16x32_bf16 v[108:111], v[28:31], v[156:159], v[108:111]\n"
    "v_mfma_f32_16x16x32_bf16 v[108:111], v[52:55], v[160:163], v[108:111]\n"
    "v_mfma_f32_16x16x32_bf16 v[108:111], v[76:79], v[164:167], v[108:111]\n"
    "v_mfma_f32_16x16x32_bf16 v[112:115], a[176:179], v[172:175], v[112:115]\n"
    "v_fma_f32 v104, v104, v176, v184\n"
    "v_fma_f32 v105, v105, v177, v185\n"
    "v_mfma_f32_16x16x32_bf16 v[112:115], a[200:203], v[144:147], v[112:115]\n"
    "v_exp_f32_e32 v192, v104\n"
    "v_mfma_f32_16x16x32_bf16 v[112:115], a[224:227], v[148:151], v[112:115]\n"
    "v_exp_f32_e32 v193, v105\n"
    "v_mfma_f32_16x16x32_bf16 v[112:115], a[248:251], v[152:155], v[112:115]\n"
    "v_fma_f32 v192, v192, s51, s51\n"
    "v_fma_f32 v193, v193, s51, s51\n"
    "v_mfma_f32_16x16x32_bf16 v[112:115], v[16:19], v[156:159], v[112:115]\n"
    "v_rcp_f32_e32 v192, v192\n"
    "v_mfma_f32_16x16x32_bf16 v[112:115], v[40:43], v[160:163], v[112:115]\n"
    "v_rcp_f32_e32 v193, v193\n"
    "v_mfma_f32_16x16x32_bf16 v[112:115], v[64:67], v[164:167], v[112:115]\n"
    "v_mul_f32_e32 v104, v104, v192\n"
    "v_mul_f32_e32 v105, v105, v193\n"
    "v_mfma_f32_16x16x32_bf16 v[116:119], a[180:183], v[172:175], v[116:119]\n"
    "v_cvt_pk_bf16_f32 v204, v104, v105\n"
    "v_fma_f32 v106, v106, v178, v186\n"
    "v_mfma_f32_16x16x32_bf16 v[116:119], a[204:207], v[144:147], v[116:119]\n"
    "v_fma_f32 v107, v107, v179, v187\n"
    "v_mfma_f32_16x16x32_bf16 v[116:119], a[228:231], v[148:151], v[116:119]\n"
    "v_exp_f32_e32 v194, v106\n"
    "v_mfma_f32_16x16x32_bf16 v[116:119], a[252:255], v[152:155], v[116:119]\n"
    "s_waitcnt vmcnt(16)\n"
    "s_barrier\n"
    "v_mfma_f32_16x16x32_bf16 v[116:119], v[20:23], v[156:159], v[116:119]\n"
    "ds_read_b128 v[168:171], v235 offset:28672\n"
    "v_fma_f32 v194, v194, s51, s51\n"
    "v_mfma_f32_16x16x32_bf16 v[116:119], v[44:47], v[160:163], v[116:119]\n"
    "ds_read_b128 v[208:211], v242 offset:8192\n"
    "s_add_u32 s50, s49, 8960\n"
    "v_mfma_f32_16x16x32_bf16 v[116:119], v[68:71], v[164:167], v[116:119]\n"
    "v_exp_f32_e32 v195, v107\n"
    "v_mfma_f32_16x16x32_bf16 v[120:123], a[168:171], v[172:175], v[120:123]\n"
    "v_fma_f32 v195, v195, s51, s51\n"
    "v_fma_f32 v108, v108, v180, v188\n"
    "v_mfma_f32_16x16x32_bf16 v[120:123], a[192:195], v[144:147], v[120:123]\n"
    "v_rcp_f32_e32 v194, v194\n"
    "v_mfma_f32_16x16x32_bf16 v[120:123], a[216:219], v[148:151], v[120:123]\n"
    "v_rcp_f32_e32 v195, v195\n"
    "v_mfma_f32_16x16x32_bf16 v[120:123], a[240:243], v[152:155], v[120:123]\n"
    "v_mul_f32_e32 v106, v106, v194\n"
    "v_mul_f32_e32 v107, v107, v195\n"
    "v_mfma_f32_16x16x32_bf16 v[120:123], v[8:11], v[156:159], v[120:123]\n"
    "v_cvt_pk_bf16_f32 v205, v106, v107\n"
    "s_waitcnt lgkmcnt(0)\n"
    "v_mfma_f32_16x16x32_bf16 v[120:123], v[32:35], v[160:163], v[120:123]\n"
    "buffer_store_dwordx4 v[208:211], v243, s[16:19], s50 offen nt\n"
    "v_mfma_f32_16x16x32_bf16 v[120:123], v[56:59], v[164:167], v[120:123]\n"
    "v_fma_f32 v109, v109, v181, v189\n"
    "v_fma_f32 v110, v110, v182, v190\n"
    "v_mfma_f32_16x16x32_bf16 v[124:127], a[172:175], v[172:175], v[124:127]\n"
    "ds_read_b128 v[172:175], v238 offset:28672\n"
    "v_fma_f32 v111, v111, v183, v191\n"
    "v_mfma_f32_16x16x32_bf16 v[124:127], a[196:199], v[144:147], v[124:127]\n"
    "ds_read_b128 v[144:147], v235 offset:30720\n"
    "v_mfma_f32_16x16x32_bf16 v[124:127], a[220:223], v[148:151], v[124:127]\n"
    "ds_read_b128 v[148:151], v238 offset:30720\n"
    "v_mfma_f32_16x16x32_bf16 v[124:127], a[244:247], v[152:155], v[124:127]\n"
    "ds_read_b128 v[152:155], v235 offset:32768\n"
    "v_mfma_f32_16x16x32_bf16 v[124:127], v[12:15], v[156:159], v[124:127]\n"
    "ds_read_b128 v[156:159], v238 offset:32768\n"
    "v_mfma_f32_16x16x32_bf16 v[124:127], v[36:39], v[160:163], v[124:127]\n"
    "ds_read_b128 v[160:163], v235 offset:34816\n"
    "v_mfma_f32_16x16x32_bf16 v[124:127], v[60:63], v[164:167], v[124:127]\n"
    "v_exp_f32_e32 v196, v108\n"
    "v_mfma_f32_16x16x32_bf16 v[112:115], a[16:19], v[168:171], v[112:115]\n"
    "v_fma_f32 v196, v196, s51, s51\n"
    "s_waitcnt lgkmcnt(4)\n"
    "v_mfma_f32_16x16x32_bf16 v[112:115], a[40:43], v[172:175], v[112:115]\n"
    "v_exp_f32_e32 v197, v109\n"
    "v_mfma_f32_16x16x32_bf16 v[112:115], a[64:67], v[144:147], v[112:115]\n"
    "v_fma_f32 v197, v197, s51, s51\n"
    "s_waitcnt lgkmcnt(2)\n"
    "v_mfma_f32_16x16x32_bf16 v[112:115], a[88:91], v[148:151], v[112:115]\n"
    "v_rcp_f32_e32 v196, v196\n"
    "v_mfma_f32_16x16x32_bf16 v[112:115], a[112:115], v[152:155], v[112:115]\n"
    "v_mul_f32_e32 v108, v108, v196\n"
    "s_waitcnt lgkmcnt(0)\n"
    "v_mfma_f32_16x16x32_bf16 v[112:115], a[136:139], v[156:159], v[112:115]\n"
    "v_rcp_f32_e32 v197, v197\n"
    "v_mfma_f32_16x16x32_bf16 v[112:115], a[160:163], v[160:163], v[112:115]\n"
    "v_mul_f32_e32 v109, v109, v197\n"
    "v_cvt_pk_bf16_f32 v206, v108, v109\n"
    "v_mfma_f32_16x16x32_bf16 v[116:119], a[20:23], v[168:171], v[116:119]\n"
    "v_exp_f32_e32 v198, v110\n"
    "v_mfma_f32_16x16x32_bf16 v[116:119], a[44:47], v[172:175], v[116:119]\n"
    "v_exp_f32_e32 v199, v111\n"
    "v_mfma_f32_16x16x32_bf16 v[116:119], a[68:71], v[144:147], v[116:119]\n"
    "v_fma_f32 v198, v198, s51, s51\n"
    "v_fma_f32 v199, v199, s51, s51\n"
    "v_mfma_f32_16x16x32_bf16 v[116:119], a[92:95], v[148:151], v[116:119]\n"
    "v_rcp_f32_e32 v198, v198\n"
    "v_mfma_f32_16x16x32_bf16 v[116:119], a[116:119], v[152:155], v[116:119]\n"
    "v_rcp_f32_e32 v199, v199\n"
    "v_mfma_f32_16x16x32_bf16 v[116:119], a[140:143], v[156:159], v[116:119]\n"
    "v_mul_f32_e32 v110, v110, v198\n"
    "v_mul_f32_e32 v111, v111, v199\n"
    "v_mfma_f32_16x16x32_bf16 v[116:119], a[164:167], v[160:163], v[116:119]\n"
    "v_cvt_pk_bf16_f32 v207, v110, v111\n"
    "v_mfma_f32_16x16x32_bf16 v[120:123], a[8:11], v[168:171], v[120:123]\n"
    "ds_write_b128 v241, v[204:207] offset:12288\n"
    "v_mfma_f32_16x16x32_bf16 v[120:123], a[32:35], v[172:175], v[120:123]\n"
    "v_mfma_f32_16x16x32_bf16 v[120:123], a[56:59], v[144:147], v[120:123]\n"
    "v_mfma_f32_16x16x32_bf16 v[120:123], a[80:83], v[148:151], v[120:123]\n"
    "v_mfma_f32_16x16x32_bf16 v[120:123], a[104:107], v[152:155], v[120:123]\n"
    "v_mfma_f32_16x16x32_bf16 v[120:123], a[128:131], v[156:159], v[120:123]\n"
    "v_mfma_f32_16x16x32_bf16 v[120:123], a[152:155], v[160:163], v[120:123]\n"
    "v_mfma_f32_16x16x32_bf16 v[124:127], a[12:15], v[168:171], v[124:127]\n"
    "v_mfma_f32_16x16x32_bf16 v[124:127], a[36:39], v[172:175], v[124:127]\n"
    "v_mfma_f32_16x16x32_bf16 v[124:127], a[60:63], v[144:147], v[124:127]\n"
    "v_mfma_f32_16x16x32_bf16 v[124:127], a[84:87], v[148:151], v[124:127]\n"
    "ds_read_b128 v[164:167], v238 offset:34816\n"
    "v_mfma_f32_16x16x32_bf16 v[124:127], a[108:111], v[152:155], v[124:127]\n"
    "v_mfma_f32_16x16x32_bf16 v[124:127], a[132:135], v[156:159], v[124:127]\n"
    "v_mfma_f32_16x16x32_bf16 v[124:127], a[156:159], v[160:163], v[124:127]\n"
    "v_mfma_f32_16x16x32_bf16 v[128:131], a[0:3], v[168:171], 0\n"
    "v_mfma_f32_16x16x32_bf16 v[128:131], a[24:27], v[172:175], v[128:131]\n"
    "v_mfma_f32_16x16x32_bf16 v[128:131], a[48:51], v[144:147], v[128:131]\n"
    "v_mfma_f32_16x16x32_bf16 v[128:131], a[72:75], v[148:151], v[128:131]\n"
    "v_mfma_f32_16x16x32_bf16 v[128:131], a[96:99], v[152:155], v[128:131]\n"
    "v_mfma_f32_16x16x32_bf16 v[128:131], a[120:123], v[156:159], v[128:131]\n"
    "v_mfma_f32_16x16x32_bf16 v[128:131], a[144:147], v[160:163], v[128:131]\n"
    "v_mfma_f32_16x16x32_bf16 v[132:135], a[4:7], v[168:171], 0\n"
    "ds_read_b128 v[168:171], v235 offset:36864\n"
    "v_mfma_f32_16x16x32_bf16 v[132:135], a[28:31], v[172:175], v[132:135]\n"
    "ds_read_b128 v[172:175], v238 offset:36864\n"
    "v_mfma_f32_16x16x32_bf16 v[132:135], a[52:55], v[144:147], v[132:135]\n"
    "ds_read_b128 v[144:147], v235 offset:38912\n"
    "v_mfma_f32_16x16x32_bf16 v[132:135], a[76:79], v[148:151], v[132:135]\n"
    "ds_read_b128 v[148:151], v238 offset:38912\n"
    "v_mfma_f32_16x16x32_bf16 v[132:135], a[100:103], v[152:155], v[132:135]\n"
    "ds_read_b128 v[152:155], v235 offset:40960\n"
    "v_mfma_f32_16x16x32_bf16 v[132:135], a[124:127], v[156:159], v[132:135]\n"
    "ds_read_b128 v[156:159], v238 offset:40960\n"
    "v_mfma_f32_16x16x32_bf16 v[132:135], a[148:151], v[160:163], v[132:135]\n"
    "s_waitcnt lgkmcnt(5)\n"
    "v_mfma_f32_16x16x32_bf16 v[112:115], a[184:187], v[164:167], v[112:115]\n"
    "v_mfma_f32_16x16x32_bf16 v[112:115], a[208:211], v[168:171], v[112:115]\n"
    "s_waitcnt lgkmcnt(3)\n"
    "v_mfma_f32_16x16x32_bf16 v[112:115], a[232:235], v[172:175], v[112:115]\n"
    "v_mfma_f32_16x16x32_bf16 v[112:115], v[0:3], v[144:147], v[112:115]\n"
    "s_waitcnt lgkmcnt(1)\n"
    "v_mfma_f32_16x16x32_bf16 v[112:115], v[24:27], v[148:151], v[112:115]\n"
    "v_mfma_f32_16x16x32_bf16 v[112:115], v[48:51], v[152:155], v[112:115]\n"
    "s_waitcnt lgkmcnt(0)\n"
    "v_mfma_f32_16x16x32_bf16 v[112:115], v[72:75], v[156:159], v[112:115]\n"
    "v_mfma_f32_16x16x32_bf16 v[116:119], a[188:191], v[164:167], v[116:119]\n"
    "v_mfma_f32_16x16x32_bf16 v[116:119], a[212:215], v[168:171], v[116:119]\n"
    "v_mfma_f32_16x16x32_bf16 v[116:119], a[236:239], v[172:175], v[116:119]\n"
    "v_mfma_f32_16x16x32_bf16 v[116:119], v[4:7], v[144:147], v[116:119]\n"
    "v_mfma_f32_16x16x32_bf16 v[116:119], v[28:31], v[148:151], v[116:119]\n"
    "v_mfma_f32_16x16x32_bf16 v[116:119], v[52:55], v[152:155], v[116:119]\n"
    "v_mfma_f32_16x16x32_bf16 v[116:119], v[76:79], v[156:159], v[116:119]\n"
    "v_mfma_f32_16x16x32_bf16 v[120:123], a[176:179], v[164:167], v[120:123]\n"
    "v_fma_f32 v112, v112, v176, v184\n"
    "v_fma_f32 v113, v113, v177, v185\n"
    "v_mfma_f32_16x16x32_bf16 v[120:123], a[200:203], v[168:171], v[120:123]\n"
    "v_exp_f32_e32 v192, v112\n"
    "v_mfma_f32_16x16x32_bf16 v[120:123], a[224:227], v[172:175], v[120:123]\n"
    "v_exp_f32_e32 v193, v113\n"
    "v_mfma_f32_16x16x32_bf16 v[120:123], a[248:251], v[144:147], v[120:123]\n"
    "v_fma_f32 v192, v192, s51, s51\n"
    "v_fma_f32 v193, v193, s51, s51\n"
    "v_mfma_f32_16x16x32_bf16 v[120:123], v[16:19], v[148:151], v[120:123]\n"
    "v_rcp_f32_e32 v192, v192\n"
    "v_mfma_f32_16x16x32_bf16 v[120:123], v[40:43], v[152:155], v[120:123]\n"
    "v_rcp_f32_e32 v193, v193\n"
    "v_mfma_f32_16x16x32_bf16 v[120:123], v[64:67], v[156:159], v[120:123]\n"
    "v_mul_f32_e32 v112, v112, v192\n"
    "v_mul_f32_e32 v113, v113, v193\n"
    "v_mfma_f32_16x16x32_bf16 v[124:127], a[180:183], v[164:167], v[124:127]\n"
    "s_add_u32 s43, s49, 10752\n"
    "v_cvt_pk_bf16_f32 v200, v112, v113\n"
    "v_mfma_f32_16x16x32_bf16 v[124:127], a[204:207], v[168:171], v[124:127]\n"
    "v_fma_f32 v114, v114, v178, v186\n"
    "v_fma_f32 v115, v115, v179, v187\n"
    "v_mfma_f32_16x16x32_bf16 v[124:127], a[228:231], v[172:175], v[124:127]\n"
    "v_exp_f32_e32 v194, v114\n"
    "v_mfma_f32_16x16x32_bf16 v[124:127], a[252:255], v[144:147], v[124:127]\n"
    "s_waitcnt vmcnt(9)\n"
    "s_barrier\n"
    "v_mfma_f32_16x16x32_bf16 v[124:127], v[20:23], v[148:151], v[124:127]\n"
    "ds_read_b128 v[160:163], v235 offset:57344\n"
    "v_fma_f32 v194, v194, s51, s51\n"
    "v_mfma_f32_16x16x32_bf16 v[124:127], v[44:47], v[152:155], v[124:127]\n"
    "ds_read_b128 v[212:215], v242 offset:12288\n"
    "s_add_u32 s50, s49, 9856\n"
    "v_mfma_f32_16x16x32_bf16 v[124:127], v[68:71], v[156:159], v[124:127]\n"
    "v_exp_f32_e32 v195, v115\n"
    "v_mfma_f32_16x16x32_bf16 v[128:131], a[168:171], v[164:167], v[128:131]\n"
    "v_fma_f32 v195, v195, s51, s51\n"
    "v_fma_f32 v116, v116, v180, v188\n"
    "v_mfma_f32_16x16x32_bf16 v[128:131], a[192:195], v[168:171], v[128:131]\n"
    "v_rcp_f32_e32 v194, v194\n"
    "v_mfma_f32_16x16x32_bf16 v[128:131], a[216:219], v[172:175], v[128:131]\n"
    "v_rcp_f32_e32 v195, v195\n"
    "v_mfma_f32_16x16x32_bf16 v[128:131], a[240:243], v[144:147], v[128:131]\n"
    "v_mul_f32_e32 v114, v114, v194\n"
    "v_mul_f32_e32 v115, v115, v195\n"
    "v_mfma_f32_16x16x32_bf16 v[128:131], v[8:11], v[148:151], v[128:131]\n"
    "v_cvt_pk_bf16_f32 v201, v114, v115\n"
    "s_waitcnt lgkmcnt(0)\n"
    "v_mfma_f32_16x16x32_bf16 v[128:131], v[32:35], v[152:155], v[128:131]\n"
    "buffer_store_dwordx4 v[212:215], v243, s[16:19], s50 offen nt\n"
    "v_mfma_f32_16x16x32_bf16 v[128:131], v[56:59], v[156:159], v[128:131]\n"
    "v_fma_f32 v117, v117, v181, v189\n"
    "v_fma_f32 v118, v118, v182, v190\n"
    "v_mfma_f32_16x16x32_bf16 v[132:135], a[172:175], v[164:167], v[132:135]\n"
    "ds_read_b128 v[164:167], v238 offset:57344\n"
    "v_fma_f32 v119, v119, v183, v191\n"
    "v_mfma_f32_16x16x32_bf16 v[132:135], a[196:199], v[168:171], v[132:135]\n"
    "ds_read_b128 v[168:171], v235 offset:59392\n"
    "v_mfma_f32_16x16x32_bf16 v[132:135], a[220:223], v[172:175], v[132:135]\n"
    "ds_read_b128 v[172:175], v238 offset:59392\n"
    "v_mfma_f32_16x16x32_bf16 v[132:135], a[244:247], v[144:147], v[132:135]\n"
    "ds_read_b128 v[144:147], v235 offset:61440\n"
    "v_mfma_f32_16x16x32_bf16 v[132:135], v[12:15], v[148:151], v[132:135]\n"
    "ds_read_b128 v[148:151], v238 offset:61440\n"
    "v_mfma_f32_16x16x32_bf16 v[132:135], v[36:39], v[152:155], v[132:135]\n"
    "ds_read_b128 v[152:155], v235 offset:63488\n"
    "v_mfma_f32_16x16x32_bf16 v[132:135], v[60:63], v[156:159], v[132:135]\n"
    "ds_read_b128 v[156:159], v238 offset:63488\n"
    "v_mfma_f32_16x16x32_bf16 v[120:123], a[16:19], v[160:163], v[120:123]\n"
    "s_waitcnt lgkmcnt(5)\n"
    "v_mfma_f32_16x16x32_bf16 v[120:123], a[40:43], v[164:167], v[120:123]\n"
    "v_exp_f32_e32 v196, v116\n"
    "v_mfma_f32_16x16x32_bf16 v[120:123], a[64:67], v[168:171], v[120:123]\n"
    "v_fma_f32 v196, v196, s51, s51\n"
    "s_waitcnt lgkmcnt(3)\n"
    "v_mfma_f32_16x16x32_bf16 v[120:123], a[88:91], v[172:175], v[120:123]\n"
    "v_exp_f32_e32 v197, v117\n"
    "v_mfma_f32_16x16x32_bf16 v[120:123], a[112:115], v[144:147], v[120:123]\n"
    "v_fma_f32 v197, v197, s51, s51\n"
    "s_waitcnt lgkmcnt(1)\n"
    "v_mfma_f32_16x16x32_bf16 v[120:123], a[136:139], v[148:151], v[120:123]\n"
    "v_rcp_f32_e32 v196, v196\n"
    "v_mfma_f32_16x16x32_bf16 v[120:123], a[160:163], v[152:155], v[120:123]\n"
    "v_rcp_f32_e32 v197, v197\n"
    "v_mfma_f32_16x16x32_bf16 v[124:127], a[20:23], v[160:163], v[124:127]\n"
    "ds_read_b128 v[160:163], v236 offset:0\n"
    "v_mul_f32_e32 v116, v116, v196\n"
    "v_mfma_f32_16x16x32_bf16 v[124:127], a[44:47], v[164:167], v[124:127]\n"
    "ds_read_b128 v[164:167], v239 offset:0\n"
    "v_mul_f32_e32 v117, v117, v197\n"
    "v_mfma_f32_16x16x32_bf16 v[124:127], a[68:71], v[168:171], v[124:127]\n"
    "ds_read_b128 v[168:171], v236 offset:2048\n"
    "v_cvt_pk_bf16_f32 v202, v116, v117\n"
    "v_mfma_f32_16x16x32_bf16 v[124:127], a[92:95], v[172:175], v[124:127]\n"
    "ds_read_b128 v[172:175], v239 offset:2048\n"
    "v_mfma_f32_16x16x32_bf16 v[124:127], a[116:119], v[144:147], v[124:127]\n"
    "ds_read_b128 v[144:147], v236 offset:4096\n"
    "v_mfma_f32_16x16x32_bf16 v[124:127], a[140:143], v[148:151], v[124:127]\n"
    "ds_read_b128 v[148:151], v239 offset:4096\n"
    "v_mfma_f32_16x16x32_bf16 v[124:127], a[164:167], v[152:155], v[124:127]\n"
    "ds_read_b128 v[152:155], v235 offset:57344\n"
    "s_waitcnt lgkmcnt(6)\n"
    "v_mfma_f32_16x16x32_bf16 v[120:123], a[184:187], v[156:159], v[120:123]\n"
    "v_exp_f32_e32 v198, v118\n"
    "v_mfma_f32_16x16x32_bf16 v[120:123], a[208:211], v[160:163], v[120:123]\n"
    "v_fma_f32 v198, v198, s51, s51\n"
    "s_waitcnt lgkmcnt(4)\n"
    "v_mfma_f32_16x16x32_bf16 v[120:123], a[232:235], v[164:167], v[120:123]\n"
    "v_exp_f32_e32 v199, v119\n"
    "v_mfma_f32_16x16x32_bf16 v[120:123], v[0:3], v[168:171], v[120:123]\n"
    "v_fma_f32 v199, v199, s51, s51\n"
    "s_waitcnt lgkmcnt(2)\n"
    "v_mfma_f32_16x16x32_bf16 v[120:123], v[24:27], v[172:175], v[120:123]\n"
    "v_rcp_f32_e32 v198, v198\n"
    "v_mfma_f32_16x16x32_bf16 v[120:123], v[48:51], v[144:147], v[120:123]\n"
    "v_mul_f32_e32 v118, v118, v198\n"
    "s_waitcnt lgkmcnt(0)\n"
    "v_mfma_f32_16x16x32_bf16 v[120:123], v[72:75], v[148:151], v[120:123]\n"
    "v_rcp_f32_e32 v199, v199\n"
    "v_mfma_f32_16x16x32_bf16 v[124:127], a[188:191], v[156:159], v[124:127]\n"
    "ds_read_b128 v[156:159], v238 offset:57344\n"
    "v_mul_f32_e32 v119, v119, v199\n"
    "v_mfma_f32_16x16x32_bf16 v[124:127], a[212:215], v[160:163], v[124:127]\n"
    "ds_read_b128 v[160:163], v235 offset:59392\n"
    "v_cvt_pk_bf16_f32 v203, v118, v119\n"
    "v_mfma_f32_16x16x32_bf16 v[124:127], a[236:239], v[164:167], v[124:127]\n"
    "ds_read_b128 v[164:167], v238 offset:59392\n"
    "v_mfma_f32_16x16x32_bf16 v[124:127], v[4:7], v[168:171], v[124:127]\n"
    "ds_read_b128 v[168:171], v235 offset:61440\n"
    "v_mfma_f32_16x16x32_bf16 v[124:127], v[28:31], v[172:175], v[124:127]\n"
    "ds_read_b128 v[172:175], v238 offset:61440\n"
    "v_mfma_f32_16x16x32_bf16 v[124:127], v[52:55], v[144:147], v[124:127]\n"
    "ds_read_b128 v[144:147], v235 offset:63488\n"
    "v_mfma_f32_16x16x32_bf16 v[124:127], v[76:79], v[148:151], v[124:127]\n"
    "ds_read_b128 v[148:151], v238 offset:63488\n"
    "v_mfma_f32_16x16x32_bf16 v[128:131], a[8:11], v[152:155], v[128:131]\n"
    "v_fma_f32 v120, v120, v176, v184\n"
    "s_waitcnt lgkmcnt(5)\n"
    "v_mfma_f32_16x16x32_bf16 v[128:131], a[32:35], v[156:159], v[128:131]\n"
    "buffer_store_dwordx4 v[200:203], v252, s[16:19], s43 offen nt\n"
    "v_mfma_f32_16x16x32_bf16 v[128:131], a[56:59], v[160:163], v[128:131]\n"
    "v_fma_f32 v121, v121, v177, v185\n"
    "s_waitcnt lgkmcnt(3)\n"
    "v_mfma_f32_16x16x32_bf16 v[128:131], a[80:83], v[164:167], v[128:131]\n"
    "v_exp_f32_e32 v192, v120\n"
    "v_mfma_f32_16x16x32_bf16 v[128:131], a[104:107], v[168:171], v[128:131]\n"
    "v_fma_f32 v192, v192, s51, s51\n"
    "s_waitcnt lgkmcnt(1)\n"
    "v_mfma_f32_16x16x32_bf16 v[128:131], a[128:131], v[172:175], v[128:131]\n"
    "v_exp_f32_e32 v193, v121\n"
    "v_mfma_f32_16x16x32_bf16 v[128:131], a[152:155], v[144:147], v[128:131]\n"
    "v_fma_f32 v193, v193, s51, s51\n"
    "v_fma_f32 v122, v122, v178, v186\n"
    "v_mfma_f32_16x16x32_bf16 v[132:135], a[12:15], v[152:155], v[132:135]\n"
    "ds_read_b128 v[152:155], v236 offset:0\n"
    "s_add_u32 s43, s49, 11648\n"
    "v_mfma_f32_16x16x32_bf16 v[132:135], a[36:39], v[156:159], v[132:135]\n"
    "ds_read_b128 v[156:159], v239 offset:0\n"
    "v_fma_f32 v123, v123, v179, v187\n"
    "v_mfma_f32_16x16x32_bf16 v[132:135], a[60:63], v[160:163], v[132:135]\n"
    "ds_read_b128 v[160:163], v236 offset:2048\n"
    "v_fma_f32 v124, v124, v180, v188\n"
    "v_mfma_f32_16x16x32_bf16 v[132:135], a[84:87], v[164:167], v[132:135]\n"
    "ds_read_b128 v[164:167], v239 offset:2048\n"
    "v_fma_f32 v125, v125, v181, v189\n"
    "v_mfma_f32_16x16x32_bf16 v[132:135], a[108:111], v[168:171], v[132:135]\n"
    "ds_read_b128 v[168:171], v236 offset:4096\n"
    "v_fma_f32 v126, v126, v182, v190\n"
    "v_mfma_f32_16x16x32_bf16 v[132:135], a[132:135], v[172:175], v[132:135]\n"
    "ds_read_b128 v[172:175], v239 offset:4096\n"
    "v_fma_f32 v127, v127, v183, v191\n"
    "v_mfma_f32_16x16x32_bf16 v[132:135], a[156:159], v[144:147], v[132:135]\n"
    "s_waitcnt lgkmcnt(5)\n"
    "v_mfma_f32_16x16x32_bf16 v[128:131], a[176:179], v[148:151], v[128:131]\n"
    "v_rcp_f32_e32 v192, v192\n"
    "v_mfma_f32_16x16x32_bf16 v[128:131], a[200:203], v[152:155], v[128:131]\n"
    "v_mul_f32_e32 v120, v120, v192\n"
    "s_waitcnt lgkmcnt(3)\n"
    "v_mfma_f32_16x16x32_bf16 v[128:131], a[224:227], v[156:159], v[128:131]\n"
    "s_waitcnt vmcnt(3) lgkmcnt(0)\n"
    "s_barrier\n"
    "v_mfma_f32_16x16x32_bf16 v[128:131], a[248:251], v[160:163], v[128:131]\n"
    "ds_read_b128 v[144:147], v236 offset:20480\n"
    "v_mfma_f32_16x16x32_bf16 v[128:131], v[16:19], v[164:167], v[128:131]\n"
    "v_rcp_f32_e32 v193, v193\n"
    "v_mfma_f32_16x16x32_bf16 v[128:131], v[40:43], v[168:171], v[128:131]\n"
    "v_mul_f32_e32 v121, v121, v193\n"
    "v_cvt_pk_bf16_f32 v204, v120, v121\n"
    "v_mfma_f32_16x16x32_bf16 v[128:131], v[64:67], v[172:175], v[128:131]\n"
    "v_exp_f32_e32 v194, v122\n"
    "v_mfma_f32_16x16x32_bf16 v[132:135], a[180:183], v[148:151], v[132:135]\n"
    "ds_read_b128 v[148:151], v239 offset:20480\n"
    "v_fma_f32 v194, v194, s51, s51\n"
    "v_mfma_f32_16x16x32_bf16 v[132:135], a[204:207], v[152:155], v[132:135]\n"
    "ds_read_b128 v[152:155], v236 offset:22528\n"
    "v_mfma_f32_16x16x32_bf16 v[132:135], a[228:231], v[156:159], v[132:135]\n"
    "ds_read_b128 v[156:159], v239 offset:22528\n"
    "v_mfma_f32_16x16x32_bf16 v[132:135], a[252:255], v[160:163], v[132:135]\n"
    "ds_read_b128 v[160:163], v236 offset:24576\n"
    "v_mfma_f32_16x16x32_bf16 v[132:135], v[20:23], v[164:167], v[132:135]\n"
    "ds_read_b128 v[164:167], v239 offset:24576\n"
    "v_mfma_f32_16x16x32_bf16 v[132:135], v[44:47], v[168:171], v[132:135]\n"
    "ds_read_b128 v[168:171], v236 offset:26624\n"
    "v_mfma_f32_16x16x32_bf16 v[132:135], v[68:71], v[172:175], v[132:135]\n"
    "ds_read_b128 v[172:175], v239 offset:26624\n"
    "s_waitcnt lgkmcnt(6)\n"
    "v_mfma_f32_16x16x32_bf16 v[128:131], a[16:19], v[144:147], v[128:131]\n"
    "v_exp_f32_e32 v195, v123\n"
    "v_mfma_f32_16x16x32_bf16 v[128:131], a[40:43], v[148:151], v[128:131]\n"
    "v_fma_f32 v195, v195, s51, s51\n"
    "s_waitcnt lgkmcnt(4)\n"
    "v_mfma_f32_16x16x32_bf16 v[128:131], a[64:67], v[152:155], v[128:131]\n"
    "v_rcp_f32_e32 v194, v194\n"
    "v_mfma_f32_16x16x32_bf16 v[128:131], a[88:91], v[156:159], v[128:131]\n"
    "v_mul_f32_e32 v122, v122, v194\n"
    "s_waitcnt lgkmcnt(2)\n"
    "v_mfma_f32_16x16x32_bf16 v[128:131], a[112:115], v[160:163], v[128:131]\n"
    "v_rcp_f32_e32 v195, v195\n"
    "v_mfma_f32_16x16x32_bf16 v[128:131], a[136:139], v[164:167], v[128:131]\n"
    "v_mul_f32_e32 v123, v123, v195\n"
    "s_waitcnt lgkmcnt(0)\n"
    "v_mfma_f32_16x16x32_bf16 v[128:131], a[160:163], v[168:171], v[128:131]\n"
    "v_cvt_pk_bf16_f32 v205, v122, v123\n"
    "v_mfma_f32_16x16x32_bf16 v[132:135], a[20:23], v[144:147], v[132:135]\n"
    "ds_read_b128 v[144:147], v236 offset:28672\n"
    "v_mfma_f32_16x16x32_bf16 v[132:135], a[44:47], v[148:151], v[132:135]\n"
    "ds_read_b128 v[148:151], v239 offset:28672\n"
    "v_mfma_f32_16x16x32_bf16 v[132:135], a[68:71], v[152:155], v[132:135]\n"
    "ds_read_b128 v[152:155], v236 offset:30720\n"
    "v_mfma_f32_16x16x32_bf16 v[132:135], a[92:95], v[156:159], v[132:135]\n"
    "ds_read_b128 v[156:159], v239 offset:30720\n"
    "v_mfma_f32_16x16x32_bf16 v[132:135], a[116:119], v[160:163], v[132:135]\n"
    "ds_read_b128 v[160:163], v236 offset:32768\n"
    "v_mfma_f32_16x16x32_bf16 v[132:135], a[140:143], v[164:167], v[132:135]\n"
    "ds_read_b128 v[164:167], v239 offset:32768\n"
    "v_mfma_f32_16x16x32_bf16 v[132:135], a[164:167], v[168:171], v[132:135]\n"
    "v_exp_f32_e32 v196, v124\n"
    "v_mfma_f32_16x16x32_bf16 v[128:131], a[184:187], v[172:175], v[128:131]\n"
    "v_fma_f32 v196, v196, s51, s51\n"
    "s_waitcnt lgkmcnt(4)\n"
    "v_mfma_f32_16x16x32_bf16 v[128:131], a[208:211], v[144:147], v[128:131]\n"
    "v_exp_f32_e32 v197, v125\n"
    "v_mfma_f32_16x16x32_bf16 v[128:131], a[232:235], v[148:151], v[128:131]\n"
    "v_fma_f32 v197, v197, s51, s51\n"
    "s_waitcnt lgkmcnt(2)\n"
    "v_mfma_f32_16x16x32_bf16 v[128:131], v[0:3], v[152:155], v[128:131]\n"
    "v_rcp_f32_e32 v196, v196\n"
    "v_mfma_f32_16x16x32_bf16 v[128:131], v[24:27], v[156:159], v[128:131]\n"
    "v_mul_f32_e32 v124, v124, v196\n"
    "s_waitcnt lgkmcnt(0)\n"
    "v_mfma_f32_16x16x32_bf16 v[128:131], v[48:51], v[160:163], v[128:131]\n"
    "v_rcp_f32_e32 v197, v197\n"
    "v_mfma_f32_16x16x32_bf16 v[128:131], v[72:75], v[164:167], v[128:131]\n"
    "v_mul_f32_e32 v125, v125, v197\n"
    "v_cvt_pk_bf16_f32 v206, v124, v125\n"
    "v_mfma_f32_16x16x32_bf16 v[132:135], a[188:191], v[172:175], v[132:135]\n"
    "v_exp_f32_e32 v198, v126\n"
    "v_mfma_f32_16x16x32_bf16 v[132:135], a[212:215], v[144:147], v[132:135]\n"
    "v_exp_f32_e32 v199, v127\n"
    "v_mfma_f32_16x16x32_bf16 v[132:135], a[236:239], v[148:151], v[132:135]\n"
    "v_fma_f32 v198, v198, s51, s51\n"
    "v_fma_f32 v199, v199, s51, s51\n"
    "v_mfma_f32_16x16x32_bf16 v[132:135], v[4:7], v[152:155], v[132:135]\n"
    "v_rcp_f32_e32 v198, v198\n"
    "v_mfma_f32_16x16x32_bf16 v[132:135], v[28:31], v[156:159], v[132:135]\n"
    "v_rcp_f32_e32 v199, v199\n"
    "v_mfma_f32_16x16x32_bf16 v[132:135], v[52:55], v[160:163], v[132:135]\n"
    "v_mul_f32_e32 v126, v126, v198\n"
    "v_mul_f32_e32 v127, v127, v199\n"
    "v_mfma_f32_16x16x32_bf16 v[132:135], v[76:79], v[164:167], v[132:135]\n"
    "s_nop 7\n"
    "v_cvt_pk_bf16_f32 v207, v126, v127\n"
    "v_fma_f32 v128, v128, v176, v184\n"
    "buffer_store_dwordx4 v[204:207], v252, s[16:19], s43 offen nt\n"
    "s_add_u32 s43, s49, 12544\n"
    "v_exp_f32_e32 v192, v128\n"
    "v_fma_f32 v129, v129, v177, v185\n"
    "v_exp_f32_e32 v193, v129\n"
    "v_fma_f32 v192, v192, s51, s51\n"
    "v_rcp_f32_e32 v192, v192\n"
    "v_fma_f32 v193, v193, s51, s51\n"
    "v_rcp_f32_e32 v193, v193\n"
    "v_mul_f32_e32 v128, v128, v192\n"
    "v_mul_f32_e32 v129, v129, v193\n"
    "v_cvt_pk_bf16_f32 v200, v128, v129\n"
    "v_fma_f32 v130, v130, v178, v186\n"
    "v_fma_f32 v131, v131, v179, v187\n"
    "v_exp_f32_e32 v194, v130\n"
    "v_fma_f32 v132, v132, v180, v188\n"
    "v_exp_f32_e32 v195, v131\n"
    "v_fma_f32 v194, v194, s51, s51\n"
    "v_rcp_f32_e32 v194, v194\n"
    "v_fma_f32 v195, v195, s51, s51\n"
    "v_rcp_f32_e32 v195, v195\n"
    "v_mul_f32_e32 v130, v130, v194\n"
    "v_exp_f32_e32 v196, v132\n"
    "v_mul_f32_e32 v131, v131, v195\n"
    "v_cvt_pk_bf16_f32 v201, v130, v131\n"
    "v_fma_f32 v133, v133, v181, v189\n"
    "v_exp_f32_e32 v197, v133\n"
    "v_fma_f32 v196, v196, s51, s51\n"
    "v_rcp_f32_e32 v196, v196\n"
    "v_fma_f32 v197, v197, s51, s51\n"
    "v_rcp_f32_e32 v197, v197\n"
    "v_mul_f32_e32 v132, v132, v196\n"
    "v_mul_f32_e32 v133, v133, v197\n"
    "v_cvt_pk_bf16_f32 v202, v132, v133\n"
    "v_fma_f32 v134, v134, v182, v190\n"
    "v_fma_f32 v135, v135, v183, v191\n"
    "v_exp_f32_e32 v198, v134\n"
    "v_exp_f32_e32 v199, v135\n"
    "v_fma_f32 v198, v198, s51, s51\n"
    "v_rcp_f32_e32 v198, v198\n"
    "v_fma_f32 v199, v199, s51, s51\n"
    "v_rcp_f32_e32 v199, v199\n"
    "v_mul_f32_e32 v134, v134, v198\n"
    "v_mul_f32_e32 v135, v135, v199\n"
    "v_cvt_pk_bf16_f32 v203, v134, v135\n"
    "buffer_store_dwordx4 v[200:203], v252, s[16:19], s43 offen nt\n"
    "s_endpgm\n"
    "5:\n"
    "3:\n"
    "s_and_b32 s54, s52, 1\n"
    "s_lshl_b32 s54, s54, 2\n"
    "s_add_u32 s58, s54, s53\n"
    "s_cmp_eq_u32 s58, 7\n"
    "s_cbranch_scc1 6f\n"
    "s_lshr_b32 s52, s52, 1\n"
    "s_lshl_b32 s52, s52, 4\n"
    "s_lshl_b32 s54, s56, 3\n"
    "s_add_u32 s52, s52, s54\n"
    "s_mul_i32 s48, s52, 229376\n"
    "s_add_u32 s38, s48, 12544\n"
    "s_mul_i32 s52, s56, 8192\n"
    "s_lshl_b32 s53, s57, 10\n"
    "s_add_u32 s46, s52, s53\n"
    "v_lshrrev_b32 v244, 3, v255\n"
    "v_and_b32 v245, 7, v255\n"
    "s_lshl_b32 s52, s57, 3\n"
    "v_add_u32 v246, s52, v244\n"
    "v_lshrrev_b32 v247, 1, v246\n"
    "v_xor_b32 v247, v247, v245\n"
    "v_lshlrev_b32 v247, 4, v247\n"
    "v_mul_u32_u24 v216, 14336, v246\n"
    "v_add_u32 v216, v216, v247\n"
    "v_and_b32 v247, 2, v244\n"
    "s_lshl_b32 s52, s57, 2\n"
    "v_or_b32 v247, s52, v247\n"
    "v_xor_b32 v247, v247, v245\n"
    "v_lshlrev_b32 v247, 4, v247\n"
    "s_lshl_b32 s52, s60, 3\n"
    "v_add_u32 v246, s52, v244\n"
    "v_mul_u32_u24 v246, 896, v246\n"
    "v_add_u32 v246, v246, v247\n"
    "v_add_u32 v219, 401408, v246\n"
    "v_add_u32 v220, 430080, v246\n"
    "v_add_u32 v217, 0, v246\n"
    "v_add_u32 v218, 28672, v246\n"
    "v_add_u32 v221, 802816, v246\n"
    "v_add_u32 v222, 831488, v246\n"
    "s_lshl_b32 s36, s58, 7\n"
    "s_lshl_b32 s45, s60, 10\n"
    "s_add_u32 s39, s48, 13440\n"
    "s_add_u32 s40, s48, 241920\n"
    "s_add_u32 s41, s48, 242816\n"
    "s_mov_b32 s10, 14680064\n"
    "s_mov_b32 s11, 0x20000\n"
    "s_mov_b32 s14, 1204224\n"
    "s_mov_b32 s15, 0x20000\n"
    "s_waitcnt lgkmcnt(0)\n"
    "s_mov_b32 s8, s68\n"
    "s_and_b32 s9, s69, 0xffff\n"
    "s_add_u32 m0, s46, 135168\n"
    "s_nop 0\n"
    "buffer_load_dwordx4 v216, s[8:11], s38 offen offset:0 lds\n"
    "s_mov_b32 s12, s70\n"
    "s_and_b32 s13, s71, 0xffff\n"
    "s_add_u32 s37, s36, 0\n"
    "s_add_u32 m0, s45, 118784\n"
    "s_nop 0\n"
    "buffer_load_dwordx4 v219, s[12:15], s37 offen lds\n"
    "s_add_u32 m0, s45, 110592\n"
    "s_add_u32 s20, s62, 3b-8b\n"
    "s_addc_u32 s21, s63, 0\n"
    "s_and_b32 s21, s21, 0xffff\n"
    "buffer_load_dwordx4 v217, s[12:15], s37 offen lds\n"
    "s_add_u32 m0, s46, 137216\n"
    "s_mov_b32 s22, 9f-3b\n"
    "s_mov_b32 s23, 0x20000\n"
    "s_mul_i32 s52, s59, 7\n"
    "buffer_load_dwordx4 v216, s[8:11], s39 offen offset:0 lds\n"
    "s_add_u32 m0, s46, 139264\n"
    "v_add_u32 v250, s52, v255\n"
    "v_lshlrev_b32 v250, 7, v250\n"
    "s_lshl_b32 s52, s61, 2\n"
    "buffer_load_dwordx4 v216, s[8:11], s40 offen offset:0 lds\n"
    "s_add_u32 m0, s46, 141312\n"
    "s_add_u32 s52, s52, s60\n"
    "s_mul_i32 s52, s52, 8\n"
    "v_add_u32 v210, s52, v255\n"
    "buffer_load_dwordx4 v216, s[8:11], s41 offen offset:0 lds\n"
    "s_add_u32 m0, s45, 122880\n"
    "v_mul_u32_u24 v211, 6242, v210\n"
    "v_lshrrev_b32 v211, 24, v211\n"
    "v_add_u32 v211, 1, v211\n"
    "buffer_load_dwordx4 v220, s[12:15], s37 offen lds\n"
    "s_add_u32 m0, s45, 114688\n"
    "v_mul_u32_u24 v211, 448, v211\n"
    "v_add_u32 v210, v210, v211\n"
    "v_lshlrev_b32 v210, 7, v210\n"
    "buffer_load_dwordx4 v218, s[12:15], s37 offen lds\n"
    "s_mov_b64 exec, 0x7f\n"
    "buffer_load_dword v253, v250, s[20:23], 0 offen\n"
    "s_mov_b64 exec, -1\n"
    "s_mov_b64 exec, 0xff\n"
    "buffer_load_dword v253, v210, s[12:15], 0 offen\n"
    "s_mov_b64 exec, -1\n"
    "s_add_u32 m0, s46, 53120\n"
    "v_lshrrev_b32 v244, 4, v255\n"
    "v_bfe_u32 v245, v255, 2, 2\n"
    "v_and_b32 v246, 3, v255\n"
    "v_and_b32 v248, 2, v245\n"
    "v_and_b32 v249, 1, v244\n"
    "v_lshl_or_b32 v248, v249, 2, v248\n"
    "s_lshl_b32 s52, s57, 2\n"
    "v_or_b32 v249, s52, v246\n"
    "v_xor_b32 v249, v249, v248\n"
    "v_lshlrev_b32 v249, 4, v249\n"
    "v_lshl_add_u32 v249, v244, 10, v249\n"
    "v_lshl_add_u32 v249, v245, 7, v249\n"
    "v_add_u32 v223, 0, v249\n"
    "v_add_u32 v224, 65536, v249\n"
    "buffer_load_dwordx4 v216, s[8:11], s38 offen offset:128 lds\n"
    "s_add_u32 s37, s36, 57344\n"
    "s_add_u32 m0, s45, 36864\n"
    "v_add_u32 v226, 8, v249\n"
    "v_add_u32 v227, 65544, v249\n"
    "v_and_b32 v247, 15, v255\n"
    "v_lshrrev_b32 v248, 1, v247\n"
    "v_xor_b32 v248, v248, v244\n"
    "v_lshlrev_b32 v248, 4, v248\n"
    "v_and_b32 v249, 7, v247\n"
    "v_lshl_add_u32 v248, v249, 7, v248\n"
    "v_lshrrev_b32 v249, 3, v247\n"
    "v_lshl_add_u32 v248, v249, 10, v248\n"
    "v_xor_b32 v249, 64, v248\n"
    "s_mul_i32 s52, s56, 8192\n"
    "s_mul_i32 s53, s56, 14336\n"
    "s_lshl_b32 s55, s57, 10\n"
    "buffer_load_dwordx4 v219, s[12:15], s37 offen lds\n"
    "s_add_u32 m0, s45, 28672\n"
    "s_add_u32 s47, s53, s55\n"
    "s_add_u32 s54, s52, 0\n"
    "v_add_u32 v229, s54, v248\n"
    "s_add_u32 s54, s52, 65536\n"
    "v_add_u32 v230, s54, v248\n"
    "s_add_u32 s54, s52, 131072\n"
    "v_add_u32 v231, s54, v248\n"
    "s_add_u32 s54, s53, 0\n"
    "v_add_u32 v235, s54, v248\n"
    "s_add_u32 s54, s53, 65536\n"
    "v_add_u32 v236, s54, v248\n"
    "s_add_u32 s54, s52, 0\n"
    "v_add_u32 v232, s54, v249\n"
    "s_add_u32 s54, s52, 65536\n"
    "buffer_load_dwordx4 v217, s[12:15], s37 offen lds\n"
    "s_add_u32 m0, s46, 55168\n"
    "v_add_u32 v233, s54, v249\n"
    "s_add_u32 s54, s52, 131072\n"
    "v_add_u32 v234, s54, v249\n"
    "s_add_u32 s54, s53, 0\n"
    "v_add_u32 v238, s54, v249\n"
    "s_add_u32 s54, s53, 65536\n"
    "v_add_u32 v239, s54, v249\n"
    "s_lshl_b32 s52, s57, 2\n"
    "v_or_b32 v248, s52, v244\n"
    "v_and_b32 v249, 7, v247\n"
    "v_xor_b32 v248, v248, v249\n"
    "v_lshlrev_b32 v248, 4, v248\n"
    "v_lshl_add_u32 v248, v247, 7, v248\n"
    "s_lshl_b32 s52, s56, 11\n"
    "buffer_load_dwordx4 v216, s[8:11], s39 offen offset:128 lds\n"
    "s_add_u32 m0, s46, 57216\n"
    "s_add_u32 s52, s52, 147456\n"
    "v_add_u32 v241, s52, v248\n"
    "s_lshl_b32 s53, s57, 10\n"
    "s_add_u32 s52, s52, s53\n"
    "v_lshlrev_b32 v248, 4, v255\n"
    "v_add_u32 v242, s52, v248\n"
    "v_lshrrev_b32 v248, 3, v255\n"
    "v_and_b32 v250, 7, v255\n"
    "v_xor_b32 v249, v248, v250\n"
    "v_lshlrev_b32 v249, 4, v249\n"
    "s_lshl_b32 s52, s57, 3\n"
    "v_add_u32 v248, s52, v248\n"
    "v_mul_u32_u24 v248, 14336, v248\n"
    "v_add_u32 v243, v248, v249\n"
    "buffer_load_dwordx4 v216, s[8:11], s40 offen offset:128 lds\n"
    "s_add_u32 m0, s46, 59264\n"
    "s_lshl_b32 s52, s57, 6\n"
    "v_lshlrev_b32 v248, 4, v244\n"
    "v_add_u32 v254, s52, v248\n"
    "v_mul_u32_u24 v248, 14336, v247\n"
    "v_add_u32 v252, v248, v254\n"
    "s_mov_b32 s16, s80\n"
    "s_and_b32 s17, s81, 0xffff\n"
    "s_mov_b32 s18, 14680064\n"
    "s_mov_b32 s19, 0x20000\n"
    "s_add_u32 s49, s48, s36\n"
    "s_mov_b32 s51, 0xbfb8aa3b\n"
    "s_mov_b32 s20, s72\n"
    "s_and_b32 s21, s73, 0xffff\n"
    "s_mov_b32 s22, 896\n"
    "buffer_load_dwordx4 v216, s[8:11], s41 offen offset:128 lds\n"
    "s_add_u32 m0, s45, 40960\n"
    "s_mov_b32 s23, 0x20000\n"
    "s_mov_b32 s24, s74\n"
    "s_and_b32 s25, s75, 0xffff\n"
    "s_mov_b32 s26, 896\n"
    "s_mov_b32 s27, 0x20000\n"
    "s_mov_b32 s28, s76\n"
    "s_and_b32 s29, s77, 0xffff\n"
    "s_mov_b32 s30, 896\n"
    "s_mov_b32 s31, 0x20000\n"
    "s_mov_b32 s64, s78\n"
    "s_and_b32 s65, s79, 0xffff\n"
    "s_mov_b32 s66, 896\n"
    "s_mov_b32 s67, 0x20000\n"
    "buffer_load_dwordx4 v220, s[12:15], s37 offen lds\n"
    "s_add_u32 m0, s45, 32768\n"
    "s_nop 0\n"
    "buffer_load_dwordx4 v218, s[12:15], s37 offen lds\n"
    "buffer_load_dwordx4 v[192:195], v254, s[20:23], s36 offen\n"
    "buffer_load_dwordx4 v[196:199], v254, s[24:27], s36 offen\n"
    "buffer_load_dwordx4 v[200:203], v254, s[28:31], s36 offen\n"
    "buffer_load_dwordx4 v[204:207], v254, s[64:67], s36 offen\n"
    "s_waitcnt vmcnt(14)\n"
    "s_barrier\n"
    "s_add_u32 m0, s46, 93952\n"
    "s_nop 0\n"
    "buffer_load_dwordx4 v216, s[8:11], s38 offen offset:256 lds\n"
    "s_add_u32 s37, s36, 114688\n"
    "s_add_u32 m0, s45, 77824\n"
    "s_nop 0\n"
    "buffer_load_dwordx4 v219, s[12:15], s37 offen lds\n"
    "s_add_u32 m0, s45, 69632\n"
    "s_nop 0\n"
    "buffer_load_dwordx4 v217, s[12:15], s37 offen lds\n"
    "s_add_u32 m0, s46, 96000\n"
    "s_nop 0\n"
    "buffer_load_dwordx4 v216, s[8:11], s39 offen offset:256 lds\n"
    "s_add_u32 m0, s46, 98048\n"
    "s_nop 0\n"
    "buffer_load_dwordx4 v216, s[8:11], s40 offen offset:256 lds\n"
    "s_add_u32 m0, s46, 100096\n"
    "s_nop 0\n"
    "buffer_load_dwordx4 v216, s[8:11], s41 offen offset:256 lds\n"
    "s_add_u32 m0, s45, 81920\n"
    "s_nop 0\n"
    "buffer_load_dwordx4 v220, s[12:15], s37 offen lds\n"
    "s_add_u32 m0, s45, 73728\n"
    "s_nop 0\n"
    "buffer_load_dwordx4 v218, s[12:15], s37 offen lds\n"
    "ds_read_b128 v[144:147], v231 offset:4096\n"
    "ds_read_b64_tr_b16 a[0:1], v224 offset:45056\n"
    "ds_read_b64_tr_b16 a[2:3], v224 offset:45568\n"
    "ds_read_b128 v[148:151], v231 offset:6144\n"
    "ds_read_b64_tr_b16 a[8:9], v224 offset:53248\n"
    "ds_read_b64_tr_b16 a[10:11], v224 offset:53760\n"
    "ds_read_b64_tr_b16 a[4:5], v227 offset:45056\n"
    "ds_read_b64_tr_b16 a[6:7], v227 offset:45568\n"
    "ds_read_b64_tr_b16 a[12:13], v227 offset:53248\n"
    "ds_read_b64_tr_b16 a[14:15], v227 offset:53760\n"
    "ds_read_b128 v[152:155], v231 offset:8192\n"
    "ds_read_b128 v[156:159], v231 offset:10240\n"
    "s_waitcnt lgkmcnt(9)\n"
    "v_mfma_f32_16x16x32_bf16 v[80:83], a[0:3], v[144:147], 0\n"
    "ds_read_b128 v[160:163], v234 offset:4096\n"
    "ds_read_b64_tr_b16 a[24:25], v224 offset:49152\n"
    "s_waitcnt lgkmcnt(8)\n"
    "v_mfma_f32_16x16x32_bf16 v[80:83], a[8:11], v[148:151], v[80:83]\n"
    "ds_read_b64_tr_b16 a[26:27], v224 offset:49664\n"
    "ds_read_b128 v[164:167], v234 offset:6144\n"
    "s_waitcnt lgkmcnt(8)\n"
    "v_mfma_f32_16x16x32_bf16 v[84:87], a[4:7], v[144:147], 0\n"
    "ds_read_b64_tr_b16 a[32:33], v224 offset:57344\n"
    "ds_read_b64_tr_b16 a[34:35], v224 offset:57856\n"
    "s_waitcnt lgkmcnt(8)\n"
    "v_mfma_f32_16x16x32_bf16 v[84:87], a[12:15], v[148:151], v[84:87]\n"
    "ds_read_b64_tr_b16 a[28:29], v227 offset:49152\n"
    "ds_read_b64_tr_b16 a[30:31], v227 offset:49664\n"
    "s_waitcnt lgkmcnt(9)\n"
    "v_mfma_f32_16x16x32_bf16 v[88:91], a[0:3], v[152:155], 0\n"
    "ds_read_b64_tr_b16 a[36:37], v227 offset:57344\n"
    "ds_read_b64_tr_b16 a[38:39], v227 offset:57856\n"
    "s_waitcnt lgkmcnt(10)\n"
    "v_mfma_f32_16x16x32_bf16 v[88:91], a[8:11], v[156:159], v[88:91]\n"
    "ds_read_b128 v[168:171], v234 offset:8192\n"
    "ds_read_b128 v[172:175], v234 offset:10240\n"
    "v_mfma_f32_16x16x32_bf16 v[92:95], a[4:7], v[152:155], 0\n"
    "s_waitcnt vmcnt(12) lgkmcnt(0)\n"
    "s_barrier\n"
    "s_add_u32 m0, s46, 134784\n"
    "s_nop 0\n"
    "buffer_load_dwordx4 v216, s[8:11], s38 offen offset:384 lds\n"
    "s_add_u32 s37, s36, 172032\n"
    "s_add_u32 m0, s45, 118784\n"
    "s_nop 0\n"
    "buffer_load_dwordx4 v219, s[12:15], s37 offen lds\n"
    "s_add_u32 m0, s45, 110592\n"
    "s_nop 0\n"
    "buffer_load_dwordx4 v217, s[12:15], s37 offen lds\n"
    "s_add_u32 m0, s46, 136832\n"
    "s_nop 0\n"
    "buffer_load_dwordx4 v216, s[8:11], s39 offen offset:384 lds\n"
    "s_add_u32 m0, s46, 138880\n"
    "s_nop 0\n"
    "buffer_load_dwordx4 v216, s[8:11], s40 offen offset:384 lds\n"
    "s_add_u32 m0, s46, 140928\n"
    "s_nop 0\n"
    "buffer_load_dwordx4 v216, s[8:11], s41 offen offset:384 lds\n"
    "s_add_u32 m0, s45, 122880\n"
    "s_nop 0\n"
    "buffer_load_dwordx4 v220, s[12:15], s37 offen lds\n"
    "s_add_u32 m0, s45, 114688\n"
    "s_nop 0\n"
    "buffer_load_dwordx4 v218, s[12:15], s37 offen lds\n"
    "v_mfma_f32_16x16x32_bf16 v[92:95], a[12:15], v[156:159], v[92:95]\n"
    "ds_read_b128 v[144:147], v229 offset:53248\n"
    "ds_read_b64_tr_b16 a[48:49], v223 offset:28672\n"
    "v_mfma_f32_16x16x32_bf16 v[80:83], a[24:27], v[160:163], v[80:83]\n"
    "ds_read_b64_tr_b16 a[50:51], v223 offset:29184\n"
    "ds_read_b128 v[148:151], v229 offset:55296\n"
    "v_mfma_f32_16x16x32_bf16 v[80:83], a[32:35], v[164:167], v[80:83]\n"
    "ds_read_b64_tr_b16 a[56:57], v223 offset:36864\n"
    "ds_read_b64_tr_b16 a[58:59], v223 offset:37376\n"
    "v_mfma_f32_16x16x32_bf16 v[84:87], a[28:31], v[160:163], v[84:87]\n"
    "ds_read_b64_tr_b16 a[52:53], v226 offset:28672\n"
    "ds_read_b64_tr_b16 a[54:55], v226 offset:29184\n"
    "v_mfma_f32_16x16x32_bf16 v[84:87], a[36:39], v[164:167], v[84:87]\n"
    "ds_read_b64_tr_b16 a[60:61], v226 offset:36864\n"
    "ds_read_b64_tr_b16 a[62:63], v226 offset:37376\n"
    "v_mfma_f32_16x16x32_bf16 v[88:91], a[24:27], v[168:171], v[88:91]\n"
    "ds_read_b128 v[152:155], v229 offset:57344\n"
    "ds_read_b128 v[156:159], v229 offset:59392\n"
    "v_mfma_f32_16x16x32_bf16 v[88:91], a[32:35], v[172:175], v[88:91]\n"
    "ds_read_b128 v[160:163], v232 offset:53248\n"
    "ds_read_b64_tr_b16 a[72:73], v223 offset:32768\n"
    "v_mfma_f32_16x16x32_bf16 v[92:95], a[28:31], v[168:171], v[92:95]\n"
    "ds_read_b64_tr_b16 a[74:75], v223 offset:33280\n"
    "ds_read_b128 v[164:167], v232 offset:55296\n"
    "v_mfma_f32_16x16x32_bf16 v[92:95], a[36:39], v[172:175], v[92:95]\n"
    "ds_read_b64_tr_b16 a[80:81], v223 offset:40960\n"
    "ds_read_b64_tr_b16 a[82:83], v223 offset:41472\n"
    "s_waitcnt lgkmcnt(12)\n"
    "v_mfma_f32_16x16x32_bf16 v[80:83], a[48:51], v[144:147], v[80:83]\n"
    "ds_read_b64_tr_b16 a[76:77], v226 offset:32768\n"
    "ds_read_b64_tr_b16 a[78:79], v226 offset:33280\n"
    "v_mfma_f32_16x16x32_bf16 v[80:83], a[56:59], v[148:151], v[80:83]\n"
    "ds_read_b64_tr_b16 a[84:85], v226 offset:40960\n"
    "ds_read_b64_tr_b16 a[86:87], v226 offset:41472\n"
    "s_waitcnt lgkmcnt(12)\n"
    "v_mfma_f32_16x16x32_bf16 v[84:87], a[52:55], v[144:147], v[84:87]\n"
    "ds_read_b128 v[168:171], v232 offset:57344\n"
    "ds_read_b128 v[172:175], v232 offset:59392\n"
    "v_mfma_f32_16x16x32_bf16 v[84:87], a[60:63], v[148:151], v[84:87]\n"
    "s_waitcnt lgkmcnt(10)\n"
    "v_mfma_f32_16x16x32_bf16 v[88:91], a[48:51], v[152:155], v[88:91]\n"
    "v_mfma_f32_16x16x32_bf16 v[88:91], a[56:59], v[156:159], v[88:91]\n"
    "s_waitcnt vmcnt(8) lgkmcnt(0)\n"
    "s_barrier\n"
    "s_add_u32 m0, s46, 52736\n"
    "s_nop 0\n"
    "buffer_load_dwordx4 v216, s[8:11], s38 offen offset:512 lds\n"
    "s_add_u32 s37, s36, 229376\n"
    "s_add_u32 m0, s45, 36864\n"
    "s_nop 0\n"
    "buffer_load_dwordx4 v219, s[12:15], s37 offen lds\n"
    "s_add_u32 m0, s45, 28672\n"
    "s_nop 0\n"
    "buffer_load_dwordx4 v217, s[12:15], s37 offen lds\n"
    "s_add_u32 m0, s46, 54784\n"
    "s_nop 0\n"
    "buffer_load_dwordx4 v216, s[8:11], s39 offen offset:512 lds\n"
    "s_add_u32 m0, s46, 56832\n"
    "s_nop 0\n"
    "buffer_load_dwordx4 v216, s[8:11], s40 offen offset:512 lds\n"
    "s_add_u32 m0, s46, 58880\n"
    "s_nop 0\n"
    "buffer_load_dwordx4 v216, s[8:11], s41 offen offset:512 lds\n"
    "s_add_u32 m0, s45, 40960\n"
    "s_nop 0\n"
    "buffer_load_dwordx4 v220, s[12:15], s37 offen lds\n"
    "s_add_u32 m0, s45, 32768\n"
    "s_nop 0\n"
    "buffer_load_dwordx4 v218, s[12:15], s37 offen lds\n"
    "s_add_u32 s44, s48, 471296\n"
    "s_add_u32 m0, s47, 0\n"
    "s_nop 0\n"
    "buffer_load_dwordx4 v216, s[8:11], s44 offen offset:0 lds\n"
    "s_add_u32 m0, s47, 1920\n"
    "s_nop 0\n"
    "buffer_load_dwordx4 v216, s[8:11], s44 offen offset:128 lds\n"
    "s_add_u32 m0, s47, 3840\n"
    "s_nop 0\n"
    "buffer_load_dwordx4 v216, s[8:11], s44 offen offset:256 lds\n"
    "s_add_u32 m0, s47, 5760\n"
    "s_nop 0\n"
    "buffer_load_dwordx4 v216, s[8:11], s44 offen offset:384 lds\n"
    "s_add_u32 m0, s47, 7680\n"
    "s_nop 0\n"
    "buffer_load_dwordx4 v216, s[8:11], s44 offen offset:512 lds\n"
    "s_add_u32 m0, s47, 9600\n"
    "s_nop 0\n"
    "buffer_load_dwordx4 v216, s[8:11], s44 offen offset:640 lds\n"
    "s_add_u32 m0, s47, 11520\n"
    "s_nop 0\n"
    "buffer_load_dwordx4 v216, s[8:11], s44 offen offset:768 lds\n"
    "v_mfma_f32_16x16x32_bf16 v[92:95], a[52:55], v[152:155], v[92:95]\n"
    "ds_read_b128 v[144:147], v230 offset:28672\n"
    "ds_read_b64_tr_b16 a[96:97], v224 offset:4096\n"
    "v_mfma_f32_16x16x32_bf16 v[92:95], a[60:63], v[156:159], v[92:95]\n"
    "ds_read_b64_tr_b16 a[98:99], v224 offset:4608\n"
    "ds_read_b128 v[148:151], v230 offset:30720\n"
    "v_mfma_f32_16x16x32_bf16 v[80:83], a[72:75], v[160:163], v[80:83]\n"
    "ds_read_b64_tr_b16 a[104:105], v224 offset:12288\n"
    "ds_read_b64_tr_b16 a[106:107], v224 offset:12800\n"
    "v_mfma_f32_16x16x32_bf16 v[80:83], a[80:83], v[164:167], v[80:83]\n"
    "ds_read_b64_tr_b16 a[100:101], v227 offset:4096\n"
    "ds_read_b64_tr_b16 a[102:103], v227 offset:4608\n"
    "v_mfma_f32_16x16x32_bf16 v[84:87], a[76:79], v[160:163], v[84:87]\n"
    "ds_read_b64_tr_b16 a[108:109], v227 offset:12288\n"
    "ds_read_b64_tr_b16 a[110:111], v227 offset:12800\n"
    "v_mfma_f32_16x16x32_bf16 v[84:87], a[84:87], v[164:167], v[84:87]\n"
    "ds_read_b128 v[152:155], v230 offset:32768\n"
    "ds_read_b128 v[156:159], v230 offset:34816\n"
    "v_mfma_f32_16x16x32_bf16 v[88:91], a[72:75], v[168:171], v[88:91]\n"
    "ds_read_b128 v[160:163], v233 offset:28672\n"
    "ds_read_b64_tr_b16 a[120:121], v224 offset:8192\n"
    "v_mfma_f32_16x16x32_bf16 v[88:91], a[80:83], v[172:175], v[88:91]\n"
    "ds_read_b64_tr_b16 a[122:123], v224 offset:8704\n"
    "ds_read_b128 v[164:167], v233 offset:30720\n"
    "v_mfma_f32_16x16x32_bf16 v[92:95], a[76:79], v[168:171], v[92:95]\n"
    "ds_read_b64_tr_b16 a[128:129], v224 offset:16384\n"
    "ds_read_b64_tr_b16 a[130:131], v224 offset:16896\n"
    "v_mfma_f32_16x16x32_bf16 v[92:95], a[84:87], v[172:175], v[92:95]\n"
    "ds_read_b64_tr_b16 a[124:125], v227 offset:8192\n"
    "ds_read_b64_tr_b16 a[126:127], v227 offset:8704\n"
    "s_waitcnt lgkmcnt(12)\n"
    "v_mfma_f32_16x16x32_bf16 v[80:83], a[96:99], v[144:147], v[80:83]\n"
    "ds_read_b64_tr_b16 a[132:133], v227 offset:16384\n"
    "ds_read_b64_tr_b16 a[134:135], v227 offset:16896\n"
    "v_mfma_f32_16x16x32_bf16 v[80:83], a[104:107], v[148:151], v[80:83]\n"
    "ds_read_b128 v[168:171], v233 offset:32768\n"
    "ds_read_b128 v[172:175], v233 offset:34816\n"
    "v_mfma_f32_16x16x32_bf16 v[84:87], a[100:103], v[144:147], v[84:87]\n"
    "v_lshlrev_b32_e32 v244, 16, v196\n"
    "s_waitcnt lgkmcnt(10)\n"
    "v_mfma_f32_16x16x32_bf16 v[84:87], a[108:111], v[148:151], v[84:87]\n"
    "v_add_f32_e32 v244, 0x3727c5ac, v244\n"
    "v_mfma_f32_16x16x32_bf16 v[88:91], a[96:99], v[152:155], v[88:91]\n"
    "s_waitcnt vmcnt(15) lgkmcnt(0)\n"
    "s_barrier\n"
    "s_add_u32 m0, s46, 93568\n"
    "s_nop 0\n"
    "buffer_load_dwordx4 v216, s[8:11], s38 offen offset:640 lds\n"
    "s_add_u32 s37, s36, 286720\n"
    "s_add_u32 m0, s45, 77824\n"
    "s_nop 0\n"
    "buffer_load_dwordx4 v219, s[12:15], s37 offen lds\n"
    "s_add_u32 m0, s45, 69632\n"
    "s_nop 0\n"
    "buffer_load_dwordx4 v217, s[12:15], s37 offen lds\n"
    "s_add_u32 m0, s46, 95616\n"
    "s_nop 0\n"
    "buffer_load_dwordx4 v216, s[8:11], s39 offen offset:640 lds\n"
    "s_add_u32 m0, s46, 97664\n"
    "s_nop 0\n"
    "buffer_load_dwordx4 v216, s[8:11], s40 offen offset:640 lds\n"
    "s_add_u32 m0, s46, 99712\n"
    "s_nop 0\n"
    "buffer_load_dwordx4 v216, s[8:11], s41 offen offset:640 lds\n"
    "s_add_u32 m0, s45, 81920\n"
    "s_nop 0\n"
    "buffer_load_dwordx4 v220, s[12:15], s37 offen lds\n"
    "s_add_u32 m0, s45, 73728\n"
    "s_nop 0\n"
    "buffer_load_dwordx4 v218, s[12:15], s37 offen lds\n"
    "v_mfma_f32_16x16x32_bf16 v[88:91], a[104:107], v[156:159], v[88:91]\n"
    "ds_read_b128 v[144:147], v231 offset:4096\n"
    "v_mfma_f32_16x16x32_bf16 v[92:95], a[100:103], v[152:155], v[92:95]\n"
    "ds_read_b64_tr_b16 a[144:145], v224 offset:45056\n"
    "v_mfma_f32_16x16x32_bf16 v[92:95], a[108:111], v[156:159], v[92:95]\n"
    "ds_read_b64_tr_b16 a[146:147], v224 offset:45568\n"
    "v_mfma_f32_16x16x32_bf16 v[80:83], a[120:123], v[160:163], v[80:83]\n"
    "ds_read_b128 v[148:151], v231 offset:6144\n"
    "ds_read_b64_tr_b16 a[152:153], v224 offset:53248\n"
    "v_mfma_f32_16x16x32_bf16 v[80:83], a[128:131], v[164:167], v[80:83]\n"
    "ds_read_b64_tr_b16 a[154:155], v224 offset:53760\n"
    "ds_read_b64_tr_b16 a[148:149], v227 offset:45056\n"
    "v_mfma_f32_16x16x32_bf16 v[84:87], a[124:127], v[160:163], v[84:87]\n"
    "ds_read_b64_tr_b16 a[150:151], v227 offset:45568\n"
    "ds_read_b64_tr_b16 a[156:157], v227 offset:53248\n"
    "v_mfma_f32_16x16x32_bf16 v[84:87], a[132:135], v[164:167], v[84:87]\n"
    "ds_read_b64_tr_b16 a[158:159], v227 offset:53760\n"
    "ds_read_b128 v[152:155], v231 offset:8192\n"
    "v_mfma_f32_16x16x32_bf16 v[88:91], a[120:123], v[168:171], v[88:91]\n"
    "ds_read_b128 v[156:159], v231 offset:10240\n"
    "ds_read_b128 v[160:163], v234 offset:4096\n"
    "v_mfma_f32_16x16x32_bf16 v[88:91], a[128:131], v[172:175], v[88:91]\n"
    "ds_read_b64_tr_b16 a[168:169], v224 offset:49152\n"
    "ds_read_b64_tr_b16 a[170:171], v224 offset:49664\n"
    "v_mfma_f32_16x16x32_bf16 v[92:95], a[124:127], v[168:171], v[92:95]\n"
    "ds_read_b128 v[164:167], v234 offset:6144\n"
    "ds_read_b64_tr_b16 a[176:177], v224 offset:57344\n"
    "v_mfma_f32_16x16x32_bf16 v[92:95], a[132:135], v[172:175], v[92:95]\n"
    "ds_read_b64_tr_b16 a[178:179], v224 offset:57856\n"
    "ds_read_b64_tr_b16 a[172:173], v227 offset:49152\n"
    "s_waitcnt lgkmcnt(12)\n"
    "v_mfma_f32_16x16x32_bf16 v[80:83], a[144:147], v[144:147], v[80:83]\n"
    "ds_read_b64_tr_b16 a[174:175], v227 offset:49664\n"
    "ds_read_b64_tr_b16 a[180:181], v227 offset:57344\n"
    "v_mfma_f32_16x16x32_bf16 v[80:83], a[152:155], v[148:151], v[80:83]\n"
    "ds_read_b64_tr_b16 a[182:183], v227 offset:57856\n"
    "ds_read_b128 v[168:171], v234 offset:8192\n"
    "s_waitcnt lgkmcnt(12)\n"
    "v_mfma_f32_16x16x32_bf16 v[84:87], a[148:151], v[144:147], v[84:87]\n"
    "ds_read_b128 v[172:175], v234 offset:10240\n"
    "v_mfma_f32_16x16x32_bf16 v[84:87], a[156:159], v[148:151], v[84:87]\n"
    "v_rsq_f32_e32 v244, v244\n"
    "v_mfma_f32_16x16x32_bf16 v[88:91], a[144:147], v[152:155], v[88:91]\n"
    "v_lshlrev_b32_e32 v245, 16, v200\n"
    "s_waitcnt lgkmcnt(7)\n"
    "v_mfma_f32_16x16x32_bf16 v[88:91], a[152:155], v[156:159], v[88:91]\n"
    "s_waitcnt vmcnt(15) lgkmcnt(0)\n"
    "s_barrier\n"
    "s_add_u32 m0, s46, 134400\n"
    "s_nop 0\n"
    "buffer_load_dwordx4 v216, s[8:11], s38 offen offset:768 lds\n"
    "s_add_u32 s37, s36, 344064\n"
    "s_add_u32 m0, s45, 118784\n"
    "s_nop 0\n"
    "buffer_load_dwordx4 v219, s[12:15], s37 offen lds\n"
    "s_add_u32 m0, s45, 110592\n"
    "s_nop 0\n"
    "buffer_load_dwordx4 v217, s[12:15], s37 offen lds\n"
    "s_add_u32 m0, s46, 136448\n"
    "s_nop 0\n"
    "buffer_load_dwordx4 v216, s[8:11], s39 offen offset:768 lds\n"
    "s_add_u32 m0, s46, 138496\n"
    "s_nop 0\n"
    "buffer_load_dwordx4 v216, s[8:11], s40 offen offset:768 lds\n"
    "s_add_u32 m0, s46, 140544\n"
    "s_nop 0\n"
    "buffer_load_dwordx4 v216, s[8:11], s41 offen offset:768 lds\n"
    "s_add_u32 m0, s45, 122880\n"
    "s_nop 0\n"
    "buffer_load_dwordx4 v220, s[12:15], s37 offen lds\n"
    "s_add_u32 m0, s45, 114688\n"
    "s_nop 0\n"
    "buffer_load_dwordx4 v218, s[12:15], s37 offen lds\n"
    "v_mfma_f32_16x16x32_bf16 v[92:95], a[148:151], v[152:155], v[92:95]\n"
    "ds_read_b128 v[144:147], v229 offset:53248\n"
    "v_mul_f32_e32 v245, s51, v245\n"
    "v_mfma_f32_16x16x32_bf16 v[92:95], a[156:159], v[156:159], v[92:95]\n"
    "ds_read_b64_tr_b16 a[192:193], v223 offset:28672\n"
    "v_mul_f32_e32 v176, v244, v245\n"
    "v_mfma_f32_16x16x32_bf16 v[80:83], a[168:171], v[160:163], v[80:83]\n"
    "ds_read_b64_tr_b16 a[194:195], v223 offset:29184\n"
    "ds_read_b128 v[148:151], v229 offset:55296\n"
    "v_mfma_f32_16x16x32_bf16 v[80:83], a[176:179], v[164:167], v[80:83]\n"
    "ds_read_b64_tr_b16 a[200:201], v223 offset:36864\n"
    "ds_read_b64_tr_b16 a[202:203], v223 offset:37376\n"
    "v_mfma_f32_16x16x32_bf16 v[84:87], a[172:175], v[160:163], v[84:87]\n"
    "ds_read_b64_tr_b16 a[196:197], v226 offset:28672\n"
    "ds_read_b64_tr_b16 a[198:199], v226 offset:29184\n"
    "v_mfma_f32_16x16x32_bf16 v[84:87], a[180:183], v[164:167], v[84:87]\n"
    "ds_read_b64_tr_b16 a[204:205], v226 offset:36864\n"
    "ds_read_b64_tr_b16 a[206:207], v226 offset:37376\n"
    "v_mfma_f32_16x16x32_bf16 v[88:91], a[168:171], v[168:171], v[88:91]\n"
    "ds_read_b128 v[152:155], v229 offset:57344\n"
    "ds_read_b128 v[156:159], v229 offset:59392\n"
    "v_mfma_f32_16x16x32_bf16 v[88:91], a[176:179], v[172:175], v[88:91]\n"
    "ds_read_b128 v[160:163], v232 offset:53248\n"
    "ds_read_b64_tr_b16 a[216:217], v223 offset:32768\n"
    "v_mfma_f32_16x16x32_bf16 v[92:95], a[172:175], v[168:171], v[92:95]\n"
    "ds_read_b64_tr_b16 a[218:219], v223 offset:33280\n"
    "ds_read_b128 v[164:167], v232 offset:55296\n"
    "v_mfma_f32_16x16x32_bf16 v[92:95], a[180:183], v[172:175], v[92:95]\n"
    "ds_read_b64_tr_b16 a[224:225], v223 offset:40960\n"
    "ds_read_b64_tr_b16 a[226:227], v223 offset:41472\n"
    "s_waitcnt lgkmcnt(12)\n"
    "v_mfma_f32_16x16x32_bf16 v[80:83], a[192:195], v[144:147], v[80:83]\n"
    "ds_read_b64_tr_b16 a[220:221], v226 offset:32768\n"
    "ds_read_b64_tr_b16 a[222:223], v226 offset:33280\n"
    "v_mfma_f32_16x16x32_bf16 v[80:83], a[200:203], v[148:151], v[80:83]\n"
    "ds_read_b64_tr_b16 a[228:229], v226 offset:40960\n"
    "ds_read_b64_tr_b16 a[230:231], v226 offset:41472\n"
    "s_waitcnt lgkmcnt(12)\n"
    "v_mfma_f32_16x16x32_bf16 v[84:87], a[196:199], v[144:147], v[84:87]\n"
    "ds_read_b128 v[168:171], v232 offset:57344\n"
    "ds_read_b128 v[172:175], v232 offset:59392\n"
    "v_mfma_f32_16x16x32_bf16 v[84:87], a[204:207], v[148:151], v[84:87]\n"
    "v_lshlrev_b32_e32 v244, 16, v204\n"
    "s_waitcnt lgkmcnt(10)\n"
    "v_mfma_f32_16x16x32_bf16 v[88:91], a[192:195], v[152:155], v[88:91]\n"
    "v_mul_f32_e32 v244, s51, v244\n"
    "v_lshlrev_b32_e32 v245, 16, v192\n"
    "v_mfma_f32_16x16x32_bf16 v[88:91], a[200:203], v[156:159], v[88:91]\n"
    "s_waitcnt vmcnt(8) lgkmcnt(0)\n"
    "s_barrier\n"
    "s_add_u32 s44, s48, 472192\n"
    "s_add_u32 m0, s47, 28672\n"
    "s_nop 0\n"
    "buffer_load_dwordx4 v216, s[8:11], s44 offen offset:0 lds\n"
    "s_add_u32 m0, s47, 30592\n"
    "s_nop 0\n"
    "buffer_load_dwordx4 v216, s[8:11], s44 offen offset:128 lds\n"
    "s_add_u32 m0, s47, 32512\n"
    "s_nop 0\n"
    "buffer_load_dwordx4 v216, s[8:11], s44 offen offset:256 lds\n"
    "s_add_u32 m0, s47, 34432\n"
    "s_nop 0\n"
    "buffer_load_dwordx4 v216, s[8:11], s44 offen offset:384 lds\n"
    "s_add_u32 m0, s47, 36352\n"
    "s_nop 0\n"
    "buffer_load_dwordx4 v216, s[8:11], s44 offen offset:512 lds\n"
    "s_add_u32 m0, s47, 38272\n"
    "s_nop 0\n"
    "buffer_load_dwordx4 v216, s[8:11], s44 offen offset:640 lds\n"
    "s_add_u32 m0, s47, 40192\n"
    "s_nop 0\n"
    "buffer_load_dwordx4 v216, s[8:11], s44 offen offset:768 lds\n"
    "v_mfma_f32_16x16x32_bf16 v[92:95], a[196:199], v[152:155], v[92:95]\n"
    "ds_read_b128 v[144:147], v230 offset:28672\n"
    "v_fma_f32 v184, -v245, v176, v244\n"
    "v_mfma_f32_16x16x32_bf16 v[92:95], a[204:207], v[156:159], v[92:95]\n"
    "ds_read_b64_tr_b16 a[240:241], v224 offset:4096\n"
    "v_and_b32_e32 v246, 0xffff0000, v196\n"
    "v_mfma_f32_16x16x32_bf16 v[80:83], a[216:219], v[160:163], v[80:83]\n"
    "ds_read_b64_tr_b16 a[242:243], v224 offset:4608\n"
    "v_add_f32_e32 v246, 0x3727c5ac, v246\n"
    "v_mfma_f32_16x16x32_bf16 v[80:83], a[224:227], v[164:167], v[80:83]\n"
    "ds_read_b128 v[148:151], v230 offset:30720\n"
    "ds_read_b64_tr_b16 a[248:249], v224 offset:12288\n"
    "v_mfma_f32_16x16x32_bf16 v[84:87], a[220:223], v[160:163], v[84:87]\n"
    "ds_read_b64_tr_b16 a[250:251], v224 offset:12800\n"
    "ds_read_b64_tr_b16 a[244:245], v227 offset:4096\n"
    "v_mfma_f32_16x16x32_bf16 v[84:87], a[228:231], v[164:167], v[84:87]\n"
    "ds_read_b64_tr_b16 a[246:247], v227 offset:4608\n"
    "ds_read_b64_tr_b16 a[252:253], v227 offset:12288\n"
    "v_mfma_f32_16x16x32_bf16 v[88:91], a[216:219], v[168:171], v[88:91]\n"
    "ds_read_b64_tr_b16 a[254:255], v227 offset:12800\n"
    "ds_read_b128 v[152:155], v230 offset:32768\n"
    "v_mfma_f32_16x16x32_bf16 v[88:91], a[224:227], v[172:175], v[88:91]\n"
    "ds_read_b128 v[156:159], v230 offset:34816\n"
    "ds_read_b128 v[160:163], v233 offset:28672\n"
    "v_mfma_f32_16x16x32_bf16 v[92:95], a[220:223], v[168:171], v[92:95]\n"
    "ds_read_b64_tr_b16 v[8:9], v224 offset:8192\n"
    "ds_read_b64_tr_b16 v[10:11], v224 offset:8704\n"
    "v_mfma_f32_16x16x32_bf16 v[92:95], a[228:231], v[172:175], v[92:95]\n"
    "ds_read_b128 v[164:167], v233 offset:30720\n"
    "ds_read_b64_tr_b16 v[16:17], v224 offset:16384\n"
    "s_waitcnt lgkmcnt(12)\n"
    "v_mfma_f32_16x16x32_bf16 v[80:83], a[240:243], v[144:147], v[80:83]\n"
    "ds_read_b64_tr_b16 v[18:19], v224 offset:16896\n"
    "ds_read_b64_tr_b16 v[12:13], v227 offset:8192\n"
    "s_waitcnt lgkmcnt(12)\n"
    "v_mfma_f32_16x16x32_bf16 v[80:83], a[248:251], v[148:151], v[80:83]\n"
    "ds_read_b64_tr_b16 v[14:15], v227 offset:8704\n"
    "ds_read_b64_tr_b16 v[20:21], v227 offset:16384\n"
    "s_waitcnt lgkmcnt(12)\n"
    "v_mfma_f32_16x16x32_bf16 v[84:87], a[244:247], v[144:147], v[84:87]\n"
    "ds_read_b64_tr_b16 v[22:23], v227 offset:16896\n"
    "ds_read_b128 v[168:171], v233 offset:32768\n"
    "s_waitcnt lgkmcnt(12)\n"
    "v_mfma_f32_16x16x32_bf16 v[84:87], a[252:255], v[148:151], v[84:87]\n"
    "ds_read_b128 v[172:175], v233 offset:34816\n"
    "v_mfma_f32_16x16x32_bf16 v[88:91], a[240:243], v[152:155], v[88:91]\n"
    "s_waitcnt lgkmcnt(9)\n"
    "v_mfma_f32_16x16x32_bf16 v[88:91], a[248:251], v[156:159], v[88:91]\n"
    "s_waitcnt vmcnt(7) lgkmcnt(0)\n"
    "s_barrier\n"
    "s_add_u32 s44, s48, 700672\n"
    "s_add_u32 m0, s47, 57344\n"
    "s_nop 0\n"
    "buffer_load_dwordx4 v216, s[8:11], s44 offen offset:0 lds\n"
    "s_add_u32 m0, s47, 59264\n"
    "s_nop 0\n"
    "buffer_load_dwordx4 v216, s[8:11], s44 offen offset:128 lds\n"
    "s_add_u32 m0, s47, 61184\n"
    "s_nop 0\n"
    "buffer_load_dwordx4 v216, s[8:11], s44 offen offset:256 lds\n"
    "s_add_u32 m0, s47, 63104\n"
    "s_nop 0\n"
    "buffer_load_dwordx4 v216, s[8:11], s44 offen offset:384 lds\n"
    "s_add_u32 m0, s47, 65024\n"
    "s_nop 0\n"
    "buffer_load_dwordx4 v216, s[8:11], s44 offen offset:512 lds\n"
    "s_add_u32 m0, s47, 66944\n"
    "s_nop 0\n"
    "buffer_load_dwordx4 v216, s[8:11], s44 offen offset:640 lds\n"
    "s_add_u32 m0, s47, 68864\n"
    "s_nop 0\n"
    "buffer_load_dwordx4 v216, s[8:11], s44 offen offset:768 lds\n"
    "v_mfma_f32_16x16x32_bf16 v[92:95], a[244:247], v[152:155], v[92:95]\n"
    "ds_read_b128 v[144:147], v231 offset:4096\n"
    "v_mfma_f32_16x16x32_bf16 v[92:95], a[252:255], v[156:159], v[92:95]\n"
    "ds_read_b64_tr_b16 v[32:33], v224 offset:45056\n"
    "v_mfma_f32_16x16x32_bf16 v[80:83], v[8:11], v[160:163], v[80:83]\n"
    "ds_read_b64_tr_b16 v[34:35], v224 offset:45568\n"
    "v_mfma_f32_16x16x32_bf16 v[80:83], v[16:19], v[164:167], v[80:83]\n"
    "ds_read_b128 v[148:151], v231 offset:6144\n"
    "v_mfma_f32_16x16x32_bf16 v[84:87], v[12:15], v[160:163], v[84:87]\n"
    "ds_read_b64_tr_b16 v[40:41], v224 offset:53248\n"
    "ds_read_b64_tr_b16 v[42:43], v224 offset:53760\n"
    "v_mfma_f32_16x16x32_bf16 v[84:87], v[20:23], v[164:167], v[84:87]\n"
    "ds_read_b64_tr_b16 v[36:37], v227 offset:45056\n"
    "ds_read_b64_tr_b16 v[38:39], v227 offset:45568\n"
    "v_mfma_f32_16x16x32_bf16 v[88:91], v[8:11], v[168:171], v[88:91]\n"
    "ds_read_b64_tr_b16 v[44:45], v227 offset:53248\n"
    "ds_read_b64_tr_b16 v[46:47], v227 offset:53760\n"
    "v_mfma_f32_16x16x32_bf16 v[88:91], v[16:19], v[172:175], v[88:91]\n"
    "ds_read_b128 v[152:155], v231 offset:8192\n"
    "v_mfma_f32_16x16x32_bf16 v[92:95], v[12:15], v[168:171], v[92:95]\n"
    "ds_read_b128 v[156:159], v231 offset:10240\n"
    "v_mfma_f32_16x16x32_bf16 v[92:95], v[20:23], v[172:175], v[92:95]\n"
    "ds_read_b128 v[160:163], v234 offset:4096\n"
    "ds_read_b64_tr_b16 v[56:57], v224 offset:49152\n"
    "s_waitcnt lgkmcnt(10)\n"
    "v_mfma_f32_16x16x32_bf16 v[80:83], v[32:35], v[144:147], v[80:83]\n"
    "ds_read_b64_tr_b16 v[58:59], v224 offset:49664\n"
    "ds_read_b128 v[164:167], v234 offset:6144\n"
    "s_waitcnt lgkmcnt(10)\n"
    "v_mfma_f32_16x16x32_bf16 v[80:83], v[40:43], v[148:151], v[80:83]\n"
    "ds_read_b64_tr_b16 v[64:65], v224 offset:57344\n"
    "ds_read_b64_tr_b16 v[66:67], v224 offset:57856\n"
    "s_waitcnt lgkmcnt(10)\n"
    "v_mfma_f32_16x16x32_bf16 v[84:87], v[36:39], v[144:147], v[84:87]\n"
    "ds_read_b64_tr_b16 v[60:61], v227 offset:49152\n"
    "ds_read_b64_tr_b16 v[62:63], v227 offset:49664\n"
    "s_waitcnt lgkmcnt(10)\n"
    "v_mfma_f32_16x16x32_bf16 v[84:87], v[44:47], v[148:151], v[84:87]\n"
    "ds_read_b64_tr_b16 v[68:69], v227 offset:57344\n"
    "ds_read_b64_tr_b16 v[70:71], v227 offset:57856\n"
    "s_waitcnt lgkmcnt(11)\n"
    "v_mfma_f32_16x16x32_bf16 v[88:91], v[32:35], v[152:155], v[88:91]\n"
    "ds_read_b128 v[168:171], v234 offset:8192\n"
    "ds_read_b128 v[172:175], v234 offset:10240\n"
    "s_waitcnt lgkmcnt(12)\n"
    "v_mfma_f32_16x16x32_bf16 v[88:91], v[40:43], v[156:159], v[88:91]\n"
    "s_waitcnt lgkmcnt(0)\n"
    "s_barrier\n"
    "s_add_u32 s44, s48, 701568\n"
    "s_add_u32 m0, s47, 86016\n"
    "s_nop 0\n"
    "buffer_load_dwordx4 v216, s[8:11], s44 offen offset:0 lds\n"
    "s_add_u32 m0, s47, 87936\n"
    "s_nop 0\n"
    "buffer_load_dwordx4 v216, s[8:11], s44 offen offset:128 lds\n"
    "s_add_u32 m0, s47, 89856\n"
    "s_nop 0\n"
    "buffer_load_dwordx4 v216, s[8:11], s44 offen offset:256 lds\n"
    "s_add_u32 m0, s47, 91776\n"
    "s_nop 0\n"
    "buffer_load_dwordx4 v216, s[8:11], s44 offen offset:384 lds\n"
    "s_add_u32 m0, s47, 93696\n"
    "s_nop 0\n"
    "buffer_load_dwordx4 v216, s[8:11], s44 offen offset:512 lds\n"
    "s_add_u32 m0, s47, 95616\n"
    "s_nop 0\n"
    "buffer_load_dwordx4 v216, s[8:11], s44 offen offset:640 lds\n"
    "s_add_u32 m0, s47, 97536\n"
    "s_nop 0\n"
    "buffer_load_dwordx4 v216, s[8:11], s44 offen offset:768 lds\n"
    "v_mfma_f32_16x16x32_bf16 v[92:95], v[36:39], v[152:155], v[92:95]\n"
    "ds_read_b128 v[144:147], v235 offset:0\n"
    "v_mfma_f32_16x16x32_bf16 v[92:95], v[44:47], v[156:159], v[92:95]\n"
    "ds_read_b128 v[148:151], v238 offset:0\n"
    "v_mfma_f32_16x16x32_bf16 v[80:83], v[56:59], v[160:163], v[80:83]\n"
    "ds_read_b128 v[152:155], v235 offset:2048\n"
    "v_mfma_f32_16x16x32_bf16 v[80:83], v[64:67], v[164:167], v[80:83]\n"
    "ds_read_b128 v[156:159], v238 offset:2048\n"
    "v_mfma_f32_16x16x32_bf16 v[84:87], v[60:63], v[160:163], v[84:87]\n"
    "ds_read_b128 v[160:163], v235 offset:4096\n"
    "v_mfma_f32_16x16x32_bf16 v[84:87], v[68:71], v[164:167], v[84:87]\n"
    "ds_read_b128 v[164:167], v238 offset:4096\n"
    "v_mfma_f32_16x16x32_bf16 v[88:91], v[56:59], v[168:171], v[88:91]\n"
    "v_rsq_f32_e32 v246, v246\n"
    "v_mfma_f32_16x16x32_bf16 v[88:91], v[64:67], v[172:175], v[88:91]\n"
    "v_and_b32_e32 v247, 0xffff0000, v200\n"
    "v_mul_f32_e32 v247, s51, v247\n"
    "v_mfma_f32_16x16x32_bf16 v[92:95], v[60:63], v[168:171], v[92:95]\n"
    "ds_read_b128 v[168:171], v235 offset:6144\n"
    "v_mul_f32_e32 v177, v246, v247\n"
    "v_mfma_f32_16x16x32_bf16 v[92:95], v[68:71], v[172:175], v[92:95]\n"
    "ds_read_b128 v[172:175], v238 offset:6144\n"
    "s_waitcnt lgkmcnt(4)\n"
    "v_mfma_f32_16x16x32_bf16 v[96:99], a[0:3], v[144:147], 0\n"
    "v_and_b32_e32 v246, 0xffff0000, v204\n"
    "v_mul_f32_e32 v246, s51, v246\n"
    "v_mfma_f32_16x16x32_bf16 v[96:99], a[24:27], v[148:151], v[96:99]\n"
    "v_and_b32_e32 v247, 0xffff0000, v192\n"
    "v_fma_f32 v185, -v247, v177, v246\n"
    "v_mfma_f32_16x16x32_bf16 v[96:99], a[48:51], v[152:155], v[96:99]\n"
    "v_lshlrev_b32_e32 v248, 16, v197\n"
    "v_mfma_f32_16x16x32_bf16 v[96:99], a[72:75], v[156:159], v[96:99]\n"
    "v_add_f32_e32 v248, 0x3727c5ac, v248\n"
    "s_waitcnt lgkmcnt(2)\n"
    "v_mfma_f32_16x16x32_bf16 v[96:99], a[96:99], v[160:163], v[96:99]\n"
    "v_rsq_f32_e32 v248, v248\n"
    "v_mfma_f32_16x16x32_bf16 v[96:99], a[120:123], v[164:167], v[96:99]\n"
    "v_lshlrev_b32_e32 v249, 16, v201\n"
    "s_waitcnt lgkmcnt(0)\n"
    "v_mfma_f32_16x16x32_bf16 v[96:99], a[144:147], v[168:171], v[96:99]\n"
    "v_mul_f32_e32 v249, s51, v249\n"
    "v_mul_f32_e32 v178, v248, v249\n"
    "v_mfma_f32_16x16x32_bf16 v[100:103], a[4:7], v[144:147], 0\n"
    "ds_read_b128 v[144:147], v235 offset:8192\n"
    "v_lshlrev_b32_e32 v248, 16, v205\n"
    "v_mfma_f32_16x16x32_bf16 v[100:103], a[28:31], v[148:151], v[100:103]\n"
    "ds_read_b128 v[148:151], v238 offset:8192\n"
    "v_mul_f32_e32 v248, s51, v248\n"
    "v_mfma_f32_16x16x32_bf16 v[100:103], a[52:55], v[152:155], v[100:103]\n"
    "ds_read_b128 v[152:155], v235 offset:10240\n"
    "v_lshlrev_b32_e32 v249, 16, v193\n"
    "v_mfma_f32_16x16x32_bf16 v[100:103], a[76:79], v[156:159], v[100:103]\n"
    "ds_read_b128 v[156:159], v238 offset:10240\n"
    "v_fma_f32 v186, -v249, v178, v248\n"
    "v_mfma_f32_16x16x32_bf16 v[100:103], a[100:103], v[160:163], v[100:103]\n"
    "ds_read_b128 v[160:163], v235 offset:12288\n"
    "v_and_b32_e32 v250, 0xffff0000, v197\n"
    "v_mfma_f32_16x16x32_bf16 v[100:103], a[124:127], v[164:167], v[100:103]\n"
    "ds_read_b128 v[164:167], v238 offset:12288\n"
    "v_add_f32_e32 v250, 0x3727c5ac, v250\n"
    "v_mfma_f32_16x16x32_bf16 v[100:103], a[148:151], v[168:171], v[100:103]\n"
    "v_rsq_f32_e32 v250, v250\n"
    "v_mfma_f32_16x16x32_bf16 v[96:99], a[168:171], v[172:175], v[96:99]\n"
    "v_and_b32_e32 v251, 0xffff0000, v201\n"
    "s_waitcnt lgkmcnt(4)\n"
    "v_mfma_f32_16x16x32_bf16 v[96:99], a[192:195], v[144:147], v[96:99]\n"
    "v_mul_f32_e32 v251, s51, v251\n"
    "v_mul_f32_e32 v179, v250, v251\n"
    "v_mfma_f32_16x16x32_bf16 v[96:99], a[216:219], v[148:151], v[96:99]\n"
    "s_waitcnt vmcnt(14) lgkmcnt(0)\n"
    "s_barrier\n"
    "s_add_u32 s44, s48, 930048\n"
    "s_add_u32 m0, s47, 0\n"
    "s_nop 0\n"
    "buffer_load_dwordx4 v216, s[8:11], s44 offen offset:0 lds\n"
    "s_add_u32 m0, s47, 1920\n"
    "s_nop 0\n"
    "buffer_load_dwordx4 v216, s[8:11], s44 offen offset:128 lds\n"
    "s_add_u32 m0, s47, 3840\n"
    "s_nop 0\n"
    "buffer_load_dwordx4 v216, s[8:11], s44 offen offset:256 lds\n"
    "s_add_u32 m0, s47, 5760\n"
    "s_nop 0\n"
    "buffer_load_dwordx4 v216, s[8:11], s44 offen offset:384 lds\n"
    "s_add_u32 m0, s47, 7680\n"
    "s_nop 0\n"
    "buffer_load_dwordx4 v216, s[8:11], s44 offen offset:512 lds\n"
    "s_add_u32 m0, s47, 9600\n"
    "s_nop 0\n"
    "buffer_load_dwordx4 v216, s[8:11], s44 offen offset:640 lds\n"
    "s_add_u32 m0, s47, 11520\n"
    "s_nop 0\n"
    "buffer_load_dwordx4 v216, s[8:11], s44 offen offset:768 lds\n"
    "v_mfma_f32_16x16x32_bf16 v[96:99], a[240:243], v[152:155], v[96:99]\n"
    "ds_read_b128 v[168:171], v235 offset:28672\n"
    "v_and_b32_e32 v250, 0xffff0000, v205\n"
    "v_mfma_f32_16x16x32_bf16 v[96:99], v[8:11], v[156:159], v[96:99]\n"
    "v_mul_f32_e32 v250, s51, v250\n"
    "v_and_b32_e32 v251, 0xffff0000, v193\n"
    "v_mfma_f32_16x16x32_bf16 v[96:99], v[32:35], v[160:163], v[96:99]\n"
    "v_fma_f32 v187, -v251, v179, v250\n"
    "v_lshlrev_b32_e32 v244, 16, v198\n"
    "v_mfma_f32_16x16x32_bf16 v[96:99], v[56:59], v[164:167], v[96:99]\n"
    "v_add_f32_e32 v244, 0x3727c5ac, v244\n"
    "v_mfma_f32_16x16x32_bf16 v[100:103], a[172:175], v[172:175], v[100:103]\n"
    "ds_read_b128 v[172:175], v238 offset:28672\n"
    "v_mfma_f32_16x16x32_bf16 v[100:103], a[196:199], v[144:147], v[100:103]\n"
    "ds_read_b128 v[144:147], v235 offset:30720\n"
    "v_mfma_f32_16x16x32_bf16 v[100:103], a[220:223], v[148:151], v[100:103]\n"
    "ds_read_b128 v[148:151], v238 offset:30720\n"
    "v_mfma_f32_16x16x32_bf16 v[100:103], a[244:247], v[152:155], v[100:103]\n"
    "ds_read_b128 v[152:155], v235 offset:32768\n"
    "v_mfma_f32_16x16x32_bf16 v[100:103], v[12:15], v[156:159], v[100:103]\n"
    "ds_read_b128 v[156:159], v238 offset:32768\n"
    "v_mfma_f32_16x16x32_bf16 v[100:103], v[36:39], v[160:163], v[100:103]\n"
    "ds_read_b128 v[160:163], v235 offset:34816\n"
    "v_mfma_f32_16x16x32_bf16 v[100:103], v[60:63], v[164:167], v[100:103]\n"
    "ds_read_b128 v[164:167], v238 offset:34816\n"
    "s_waitcnt lgkmcnt(6)\n"
    "v_mfma_f32_16x16x32_bf16 v[96:99], a[8:11], v[168:171], v[96:99]\n"
    "v_rsq_f32_e32 v244, v244\n"
    "v_mfma_f32_16x16x32_bf16 v[96:99], a[32:35], v[172:175], v[96:99]\n"
    "v_lshlrev_b32_e32 v245, 16, v202\n"
    "s_waitcnt lgkmcnt(4)\n"
    "v_mfma_f32_16x16x32_bf16 v[96:99], a[56:59], v[144:147], v[96:99]\n"
    "v_mul_f32_e32 v245, s51, v245\n"
    "v_mul_f32_e32 v180, v244, v245\n"
    "v_mfma_f32_16x16x32_bf16 v[96:99], a[80:83], v[148:151], v[96:99]\n"
    "v_lshlrev_b32_e32 v244, 16, v206\n"
    "s_waitcnt lgkmcnt(2)\n"
    "v_mfma_f32_16x16x32_bf16 v[96:99], a[104:107], v[152:155], v[96:99]\n"
    "v_mul_f32_e32 v244, s51, v244\n"
    "v_lshlrev_b32_e32 v245, 16, v194\n"
    "v_mfma_f32_16x16x32_bf16 v[96:99], a[128:131], v[156:159], v[96:99]\n"
    "v_fma_f32 v188, -v245, v180, v244\n"
    "s_waitcnt lgkmcnt(0)\n"
    "v_mfma_f32_16x16x32_bf16 v[96:99], a[152:155], v[160:163], v[96:99]\n"
    "v_and_b32_e32 v246, 0xffff0000, v198\n"
    "v_add_f32_e32 v246, 0x3727c5ac, v246\n"
    "v_mfma_f32_16x16x32_bf16 v[100:103], a[12:15], v[168:171], v[100:103]\n"
    "ds_read_b128 v[168:171], v235 offset:36864\n"
    "v_mfma_f32_16x16x32_bf16 v[100:103], a[36:39], v[172:175], v[100:103]\n"
    "ds_read_b128 v[172:175], v238 offset:36864\n"
    "v_mfma_f32_16x16x32_bf16 v[100:103], a[60:63], v[144:147], v[100:103]\n"
    "ds_read_b128 v[144:147], v235 offset:38912\n"
    "v_mfma_f32_16x16x32_bf16 v[100:103], a[84:87], v[148:151], v[100:103]\n"
    "ds_read_b128 v[148:151], v238 offset:38912\n"
    "v_mfma_f32_16x16x32_bf16 v[100:103], a[108:111], v[152:155], v[100:103]\n"
    "ds_read_b128 v[152:155], v235 offset:40960\n"
    "v_mfma_f32_16x16x32_bf16 v[100:103], a[132:135], v[156:159], v[100:103]\n"
    "ds_read_b128 v[156:159], v238 offset:40960\n"
    "v_mfma_f32_16x16x32_bf16 v[100:103], a[156:159], v[160:163], v[100:103]\n"
    "v_rsq_f32_e32 v246, v246\n"
    "v_mfma_f32_16x16x32_bf16 v[96:99], a[176:179], v[164:167], v[96:99]\n"
    "v_and_b32_e32 v247, 0xffff0000, v202\n"
    "s_waitcnt lgkmcnt(4)\n"
    "v_mfma_f32_16x16x32_bf16 v[96:99], a[200:203], v[168:171], v[96:99]\n"
    "v_mul_f32_e32 v247, s51, v247\n"
    "v_mul_f32_e32 v181, v246, v247\n"
    "v_mfma_f32_16x16x32_bf16 v[96:99], a[224:227], v[172:175], v[96:99]\n"
    "s_waitcnt vmcnt(14) lgkmcnt(0)\n"
    "s_barrier\n"
    "s_add_u32 s44, s48, 930944\n"
    "s_add_u32 m0, s47, 28672\n"
    "s_nop 0\n"
    "buffer_load_dwordx4 v216, s[8:11], s44 offen offset:0 lds\n"
    "s_add_u32 m0, s47, 30592\n"
    "s_nop 0\n"
    "buffer_load_dwordx4 v216, s[8:11], s44 offen offset:128 lds\n"
    "s_add_u32 m0, s47, 32512\n"
    "s_nop 0\n"
    "buffer_load_dwordx4 v216, s[8:11], s44 offen offset:256 lds\n"
    "s_add_u32 m0, s47, 34432\n"
    "s_nop 0\n"
    "buffer_load_dwordx4 v216, s[8:11], s44 offen offset:384 lds\n"
    "s_add_u32 m0, s47, 36352\n"
    "s_nop 0\n"
    "buffer_load_dwordx4 v216, s[8:11], s44 offen offset:512 lds\n"
    "s_add_u32 m0, s47, 38272\n"
    "s_nop 0\n"
    "buffer_load_dwordx4 v216, s[8:11], s44 offen offset:640 lds\n"
    "s_add_u32 m0, s47, 40192\n"
    "s_nop 0\n"
    "buffer_load_dwordx4 v216, s[8:11], s44 offen offset:768 lds\n"
    "v_mfma_f32_16x16x32_bf16 v[96:99], a[248:251], v[144:147], v[96:99]\n"
    "ds_read_b128 v[160:163], v235 offset:57344\n"
    "v_and_b32_e32 v246, 0xffff0000, v206\n"
    "v_mfma_f32_16x16x32_bf16 v[96:99], v[16:19], v[148:151], v[96:99]\n"
    "v_mul_f32_e32 v246, s51, v246\n"
    "v_and_b32_e32 v247, 0xffff0000, v194\n"
    "v_mfma_f32_16x16x32_bf16 v[96:99], v[40:43], v[152:155], v[96:99]\n"
    "v_fma_f32 v189, -v247, v181, v246\n"
    "v_lshlrev_b32_e32 v248, 16, v199\n"
    "v_mfma_f32_16x16x32_bf16 v[96:99], v[64:67], v[156:159], v[96:99]\n"
    "v_add_f32_e32 v248, 0x3727c5ac, v248\n"
    "v_mfma_f32_16x16x32_bf16 v[100:103], a[180:183], v[164:167], v[100:103]\n"
    "ds_read_b128 v[164:167], v238 offset:57344\n"
    "v_mfma_f32_16x16x32_bf16 v[100:103], a[204:207], v[168:171], v[100:103]\n"
    "ds_read_b128 v[168:171], v235 offset:59392\n"
    "v_mfma_f32_16x16x32_bf16 v[100:103], a[228:231], v[172:175], v[100:103]\n"
    "ds_read_b128 v[172:175], v238 offset:59392\n"
    "v_mfma_f32_16x16x32_bf16 v[100:103], a[252:255], v[144:147], v[100:103]\n"
    "ds_read_b128 v[144:147], v235 offset:61440\n"
    "v_mfma_f32_16x16x32_bf16 v[100:103], v[20:23], v[148:151], v[100:103]\n"
    "ds_read_b128 v[148:151], v238 offset:61440\n"
    "v_mfma_f32_16x16x32_bf16 v[100:103], v[44:47], v[152:155], v[100:103]\n"
    "ds_read_b128 v[152:155], v235 offset:63488\n"
    "v_mfma_f32_16x16x32_bf16 v[100:103], v[68:71], v[156:159], v[100:103]\n"
    "ds_read_b128 v[156:159], v238 offset:63488\n"
    "s_waitcnt lgkmcnt(6)\n"
    "v_mfma_f32_16x16x32_bf16 v[104:107], a[0:3], v[160:163], 0\n"
    "v_rsq_f32_e32 v248, v248\n"
    "v_mfma_f32_16x16x32_bf16 v[104:107], a[24:27], v[164:167], v[104:107]\n"
    "v_lshlrev_b32_e32 v249, 16, v203\n"
    "s_waitcnt lgkmcnt(4)\n"
    "v_mfma_f32_16x16x32_bf16 v[104:107], a[48:51], v[168:171], v[104:107]\n"
    "v_mul_f32_e32 v249, s51, v249\n"
    "v_mul_f32_e32 v182, v248, v249\n"
    "v_mfma_f32_16x16x32_bf16 v[104:107], a[72:75], v[172:175], v[104:107]\n"
    "v_lshlrev_b32_e32 v248, 16, v207\n"
    "s_waitcnt lgkmcnt(2)\n"
    "v_mfma_f32_16x16x32_bf16 v[104:107], a[96:99], v[144:147], v[104:107]\n"
    "v_mul_f32_e32 v248, s51, v248\n"
    "v_lshlrev_b32_e32 v249, 16, v195\n"
    "v_mfma_f32_16x16x32_bf16 v[104:107], a[120:123], v[148:151], v[104:107]\n"
    "v_fma_f32 v190, -v249, v182, v248\n"
    "s_waitcnt lgkmcnt(0)\n"
    "v_mfma_f32_16x16x32_bf16 v[104:107], a[144:147], v[152:155], v[104:107]\n"
    "v_and_b32_e32 v250, 0xffff0000, v199\n"
    "v_add_f32_e32 v250, 0x3727c5ac, v250\n"
    "v_mfma_f32_16x16x32_bf16 v[108:111], a[4:7], v[160:163], 0\n"
    "ds_read_b128 v[160:163], v236 offset:0\n"
    "v_mfma_f32_16x16x32_bf16 v[108:111], a[28:31], v[164:167], v[108:111]\n"
    "ds_read_b128 v[164:167], v239 offset:0\n"
    "v_mfma_f32_16x16x32_bf16 v[108:111], a[52:55], v[168:171], v[108:111]\n"
    "ds_read_b128 v[168:171], v236 offset:2048\n"
    "v_mfma_f32_16x16x32_bf16 v[108:111], a[76:79], v[172:175], v[108:111]\n"
    "ds_read_b128 v[172:175], v239 offset:2048\n"
    "v_mfma_f32_16x16x32_bf16 v[108:111], a[100:103], v[144:147], v[108:111]\n"
    "ds_read_b128 v[144:147], v236 offset:4096\n"
    "v_mfma_f32_16x16x32_bf16 v[108:111], a[124:127], v[148:151], v[108:111]\n"
    "ds_read_b128 v[148:151], v239 offset:4096\n"
    "v_mfma_f32_16x16x32_bf16 v[108:111], a[148:151], v[152:155], v[108:111]\n"
    "v_rsq_f32_e32 v250, v250\n"
    "v_mfma_f32_16x16x32_bf16 v[104:107], a[168:171], v[156:159], v[104:107]\n"
    "v_and_b32_e32 v251, 0xffff0000, v203\n"
    "s_waitcnt lgkmcnt(4)\n"
    "v_mfma_f32_16x16x32_bf16 v[104:107], a[192:195], v[160:163], v[104:107]\n"
    "v_mul_f32_e32 v251, s51, v251\n"
    "v_mul_f32_e32 v183, v250, v251\n"
    "v_mfma_f32_16x16x32_bf16 v[104:107], a[216:219], v[164:167], v[104:107]\n"
    "s_waitcnt vmcnt(14) lgkmcnt(0)\n"
    "s_barrier\n"
    "s_add_u32 s44, s48, 1159424\n"
    "s_add_u32 m0, s47, 57344\n"
    "s_nop 0\n"
    "buffer_load_dwordx4 v216, s[8:11], s44 offen offset:0 lds\n"
    "s_add_u32 m0, s47, 59264\n"
    "s_nop 0\n"
    "buffer_load_dwordx4 v216, s[8:11], s44 offen offset:128 lds\n"
    "s_add_u32 m0, s47, 61184\n"
    "s_nop 0\n"
    "buffer_load_dwordx4 v216, s[8:11], s44 offen offset:256 lds\n"
    "s_add_u32 m0, s47, 63104\n"
    "s_nop 0\n"
    "buffer_load_dwordx4 v216, s[8:11], s44 offen offset:384 lds\n"
    "s_add_u32 m0, s47, 65024\n"
    "s_nop 0\n"
    "buffer_load_dwordx4 v216, s[8:11], s44 offen offset:512 lds\n"
    "s_add_u32 m0, s47, 66944\n"
    "s_nop 0\n"
    "buffer_load_dwordx4 v216, s[8:11], s44 offen offset:640 lds\n"
    "s_add_u32 m0, s47, 68864\n"
    "s_nop 0\n"
    "buffer_load_dwordx4 v216, s[8:11], s44 offen offset:768 lds\n"
    "v_mfma_f32_16x16x32_bf16 v[104:107], a[240:243], v[168:171], v[104:107]\n"
    "ds_read_b128 v[152:155], v236 offset:20480\n"
    "v_and_b32_e32 v250, 0xffff0000, v207\n"
    "v_mfma_f32_16x16x32_bf16 v[104:107], v[8:11], v[172:175], v[104:107]\n"
    "v_mul_f32_e32 v250, s51, v250\n"
    "v_and_b32_e32 v251, 0xffff0000, v195\n"
    "v_mfma_f32_16x16x32_bf16 v[104:107], v[32:35], v[144:147], v[104:107]\n"
    "v_fma_f32 v191, -v251, v183, v250\n"
    "v_mfma_f32_16x16x32_bf16 v[104:107], v[56:59], v[148:151], v[104:107]\n"
    "v_fma_f32 v80, v80, v176, v184\n"
    "v_fma_f32 v81, v81, v177, v185\n"
    "v_mfma_f32_16x16x32_bf16 v[108:111], a[172:175], v[156:159], v[108:111]\n"
    "ds_read_b128 v[156:159], v239 offset:20480\n"
    "v_fma_f32 v82, v82, v178, v186\n"
    "v_mfma_f32_16x16x32_bf16 v[108:111], a[196:199], v[160:163], v[108:111]\n"
    "ds_read_b128 v[160:163], v236 offset:22528\n"
    "v_fma_f32 v83, v83, v179, v187\n"
    "v_mfma_f32_16x16x32_bf16 v[108:111], a[220:223], v[164:167], v[108:111]\n"
    "ds_read_b128 v[164:167], v239 offset:22528\n"
    "v_fma_f32 v84, v84, v180, v188\n"
    "v_mfma_f32_16x16x32_bf16 v[108:111], a[244:247], v[168:171], v[108:111]\n"
    "ds_read_b128 v[168:171], v236 offset:24576\n"
    "v_fma_f32 v85, v85, v181, v189\n"
    "v_mfma_f32_16x16x32_bf16 v[108:111], v[12:15], v[172:175], v[108:111]\n"
    "ds_read_b128 v[172:175], v239 offset:24576\n"
    "v_fma_f32 v86, v86, v182, v190\n"
    "v_mfma_f32_16x16x32_bf16 v[108:111], v[36:39], v[144:147], v[108:111]\n"
    "ds_read_b128 v[144:147], v236 offset:26624\n"
    "v_fma_f32 v87, v87, v183, v191\n"
    "v_mfma_f32_16x16x32_bf16 v[108:111], v[60:63], v[148:151], v[108:111]\n"
    "ds_read_b128 v[148:151], v239 offset:26624\n"
    "s_waitcnt lgkmcnt(6)\n"
    "v_mfma_f32_16x16x32_bf16 v[104:107], a[8:11], v[152:155], v[104:107]\n"
    "v_exp_f32_e32 v192, v80\n"
    "v_mfma_f32_16x16x32_bf16 v[104:107], a[32:35], v[156:159], v[104:107]\n"
    "v_fma_f32 v192, v192, s51, s51\n"
    "s_waitcnt lgkmcnt(4)\n"
    "v_mfma_f32_16x16x32_bf16 v[104:107], a[56:59], v[160:163], v[104:107]\n"
    "v_exp_f32_e32 v193, v81\n"
    "v_mfma_f32_16x16x32_bf16 v[104:107], a[80:83], v[164:167], v[104:107]\n"
    "v_fma_f32 v193, v193, s51, s51\n"
    "s_waitcnt lgkmcnt(2)\n"
    "v_mfma_f32_16x16x32_bf16 v[104:107], a[104:107], v[168:171], v[104:107]\n"
    "v_rcp_f32_e32 v192, v192\n"
    "v_mfma_f32_16x16x32_bf16 v[104:107], a[128:131], v[172:175], v[104:107]\n"
    "v_mul_f32_e32 v80, v80, v192\n"
    "s_waitcnt lgkmcnt(0)\n"
    "v_mfma_f32_16x16x32_bf16 v[104:107], a[152:155], v[144:147], v[104:107]\n"
    "v_rcp_f32_e32 v193, v193\n"
    "v_mfma_f32_16x16x32_bf16 v[108:111], a[12:15], v[152:155], v[108:111]\n"
    "ds_read_b128 v[152:155], v236 offset:28672\n"
    "v_mul_f32_e32 v81, v81, v193\n"
    "v_mfma_f32_16x16x32_bf16 v[108:111], a[36:39], v[156:159], v[108:111]\n"
    "ds_read_b128 v[156:159], v239 offset:28672\n"
    "v_cvt_pk_bf16_f32 v200, v80, v81\n"
    "v_mfma_f32_16x16x32_bf16 v[108:111], a[60:63], v[160:163], v[108:111]\n"
    "ds_read_b128 v[160:163], v236 offset:30720\n"
    "v_mfma_f32_16x16x32_bf16 v[108:111], a[84:87], v[164:167], v[108:111]\n"
    "ds_read_b128 v[164:167], v239 offset:30720\n"
    "v_mfma_f32_16x16x32_bf16 v[108:111], a[108:111], v[168:171], v[108:111]\n"
    "ds_read_b128 v[168:171], v236 offset:32768\n"
    "v_mfma_f32_16x16x32_bf16 v[108:111], a[132:135], v[172:175], v[108:111]\n"
    "ds_read_b128 v[172:175], v239 offset:32768\n"
    "v_mfma_f32_16x16x32_bf16 v[108:111], a[156:159], v[144:147], v[108:111]\n"
    "v_exp_f32_e32 v194, v82\n"
    "v_mfma_f32_16x16x32_bf16 v[104:107], a[176:179], v[148:151], v[104:107]\n"
    "v_fma_f32 v194, v194, s51, s51\n"
    "s_waitcnt lgkmcnt(4)\n"
    "v_mfma_f32_16x16x32_bf16 v[104:107], a[200:203], v[152:155], v[104:107]\n"
    "v_exp_f32_e32 v195, v83\n"
    "v_mfma_f32_16x16x32_bf16 v[104:107], a[224:227], v[156:159], v[104:107]\n"
    "s_waitcnt vmcnt(14) lgkmcnt(0)\n"
    "s_barrier\n"
    "s_add_u32 s44, s48, 1160320\n"
    "s_add_u32 m0, s47, 86016\n"
    "s_nop 0\n"
    "buffer_load_dwordx4 v216, s[8:11], s44 offen offset:0 lds\n"
    "s_add_u32 m0, s47, 87936\n"
    "s_nop 0\n"
    "buffer_load_dwordx4 v216, s[8:11], s44 offen offset:128 lds\n"
    "s_add_u32 m0, s47, 89856\n"
    "s_nop 0\n"
    "buffer_load_dwordx4 v216, s[8:11], s44 offen offset:256 lds\n"
    "s_add_u32 m0, s47, 91776\n"
    "s_nop 0\n"
    "buffer_load_dwordx4 v216, s[8:11], s44 offen offset:384 lds\n"
    "s_add_u32 m0, s47, 93696\n"
    "s_nop 0\n"
    "buffer_load_dwordx4 v216, s[8:11], s44 offen offset:512 lds\n"
    "s_add_u32 m0, s47, 95616\n"
    "s_nop 0\n"
    "buffer_load_dwordx4 v216, s[8:11], s44 offen offset:640 lds\n"
    "s_add_u32 m0, s47, 97536\n"
    "s_nop 0\n"
    "buffer_load_dwordx4 v216, s[8:11], s44 offen offset:768 lds\n"
    "v_mfma_f32_16x16x32_bf16 v[104:107], a[248:251], v[160:163], v[104:107]\n"
    "ds_read_b128 v[144:147], v235 offset:0\n"
    "v_fma_f32 v195, v195, s51, s51\n"
    "v_mfma_f32_16x16x32_bf16 v[104:107], v[16:19], v[164:167], v[104:107]\n"
    "v_rcp_f32_e32 v194, v194\n"
    "v_mfma_f32_16x16x32_bf16 v[104:107], v[40:43], v[168:171], v[104:107]\n"
    "v_rcp_f32_e32 v195, v195\n"
    "v_mfma_f32_16x16x32_bf16 v[104:107], v[64:67], v[172:175], v[104:107]\n"
    "v_mul_f32_e32 v82, v82, v194\n"
    "v_mul_f32_e32 v83, v83, v195\n"
    "v_mfma_f32_16x16x32_bf16 v[108:111], a[180:183], v[148:151], v[108:111]\n"
    "ds_read_b128 v[148:151], v238 offset:0\n"
    "v_cvt_pk_bf16_f32 v201, v82, v83\n"
    "v_mfma_f32_16x16x32_bf16 v[108:111], a[204:207], v[152:155], v[108:111]\n"
    "ds_read_b128 v[152:155], v235 offset:2048\n"
    "v_fma_f32 v88, v88, v176, v184\n"
    "v_mfma_f32_16x16x32_bf16 v[108:111], a[228:231], v[156:159], v[108:111]\n"
    "ds_read_b128 v[156:159], v238 offset:2048\n"
    "v_fma_f32 v89, v89, v177, v185\n"
    "v_mfma_f32_16x16x32_bf16 v[108:111], a[252:255], v[160:163], v[108:111]\n"
    "ds_read_b128 v[160:163], v235 offset:4096\n"
    "v_fma_f32 v90, v90, v178, v186\n"
    "v_mfma_f32_16x16x32_bf16 v[108:111], v[20:23], v[164:167], v[108:111]\n"
    "ds_read_b128 v[164:167], v238 offset:4096\n"
    "v_fma_f32 v91, v91, v179, v187\n"
    "v_mfma_f32_16x16x32_bf16 v[108:111], v[44:47], v[168:171], v[108:111]\n"
    "ds_read_b128 v[168:171], v235 offset:6144\n"
    "v_mfma_f32_16x16x32_bf16 v[108:111], v[68:71], v[172:175], v[108:111]\n"
    "ds_read_b128 v[172:175], v238 offset:6144\n"
    "s_waitcnt lgkmcnt(6)\n"
    "v_mfma_f32_16x16x32_bf16 v[112:115], a[0:3], v[144:147], 0\n"
    "v_exp_f32_e32 v196, v84\n"
    "v_mfma_f32_16x16x32_bf16 v[112:115], a[24:27], v[148:151], v[112:115]\n"
    "v_fma_f32 v196, v196, s51, s51\n"
    "s_waitcnt lgkmcnt(4)\n"
    "v_mfma_f32_16x16x32_bf16 v[112:115], a[48:51], v[152:155], v[112:115]\n"
    "v_exp_f32_e32 v197, v85\n"
    "v_mfma_f32_16x16x32_bf16 v[112:115], a[72:75], v[156:159], v[112:115]\n"
    "v_fma_f32 v197, v197, s51, s51\n"
    "s_waitcnt lgkmcnt(2)\n"
    "v_mfma_f32_16x16x32_bf16 v[112:115], a[96:99], v[160:163], v[112:115]\n"
    "v_rcp_f32_e32 v196, v196\n"
    "v_mfma_f32_16x16x32_bf16 v[112:115], a[120:123], v[164:167], v[112:115]\n"
    "v_mul_f32_e32 v84, v84, v196\n"
    "s_waitcnt lgkmcnt(0)\n"
    "v_mfma_f32_16x16x32_bf16 v[112:115], a[144:147], v[168:171], v[112:115]\n"
    "v_rcp_f32_e32 v197, v197\n"
    "v_mfma_f32_16x16x32_bf16 v[116:119], a[4:7], v[144:147], 0\n"
    "ds_read_b128 v[144:147], v235 offset:8192\n"
    "v_mul_f32_e32 v85, v85, v197\n"
    "v_mfma_f32_16x16x32_bf16 v[116:119], a[28:31], v[148:151], v[116:119]\n"
    "ds_read_b128 v[148:151], v238 offset:8192\n"
    "v_cvt_pk_bf16_f32 v202, v84, v85\n"
    "v_mfma_f32_16x16x32_bf16 v[116:119], a[52:55], v[152:155], v[116:119]\n"
    "ds_read_b128 v[152:155], v235 offset:10240\n"
    "v_mfma_f32_16x16x32_bf16 v[116:119], a[76:79], v[156:159], v[116:119]\n"
    "ds_read_b128 v[156:159], v238 offset:10240\n"
    "v_mfma_f32_16x16x32_bf16 v[116:119], a[100:103], v[160:163], v[116:119]\n"
    "ds_read_b128 v[160:163], v235 offset:12288\n"
    "v_mfma_f32_16x16x32_bf16 v[116:119], a[124:127], v[164:167], v[116:119]\n"
    "ds_read_b128 v[164:167], v238 offset:12288\n"
    "v_mfma_f32_16x16x32_bf16 v[116:119], a[148:151], v[168:171], v[116:119]\n"
    "v_exp_f32_e32 v198, v86\n"
    "v_mfma_f32_16x16x32_bf16 v[112:115], a[168:171], v[172:175], v[112:115]\n"
    "v_fma_f32 v198, v198, s51, s51\n"
    "s_waitcnt lgkmcnt(4)\n"
    "v_mfma_f32_16x16x32_bf16 v[112:115], a[192:195], v[144:147], v[112:115]\n"
    "v_exp_f32_e32 v199, v87\n"
    "v_mfma_f32_16x16x32_bf16 v[112:115], a[216:219], v[148:151], v[112:115]\n"
    "s_waitcnt vmcnt(14) lgkmcnt(0)\n"
    "s_barrier\n"
    "s_add_u32 s44, s48, 1388800\n"
    "s_add_u32 m0, s47, 0\n"
    "s_nop 0\n"
    "buffer_load_dwordx4 v216, s[8:11], s44 offen offset:0 lds\n"
    "s_add_u32 m0, s47, 1920\n"
    "s_nop 0\n"
    "buffer_load_dwordx4 v216, s[8:11], s44 offen offset:128 lds\n"
    "s_add_u32 m0, s47, 3840\n"
    "s_nop 0\n"
    "buffer_load_dwordx4 v216, s[8:11], s44 offen offset:256 lds\n"
    "s_add_u32 m0, s47, 5760\n"
    "s_nop 0\n"
    "buffer_load_dwordx4 v216, s[8:11], s44 offen offset:384 lds\n"
    "s_add_u32 m0, s47, 7680\n"
    "s_nop 0\n"
    "buffer_load_dwordx4 v216, s[8:11], s44 offen offset:512 lds\n"
    "s_add_u32 m0, s47, 9600\n"
    "s_nop 0\n"
    "buffer_load_dwordx4 v216, s[8:11], s44 offen offset:640 lds\n"
    "s_add_u32 m0, s47, 11520\n"
    "s_nop 0\n"
    "buffer_load_dwordx4 v216, s[8:11], s44 offen offset:768 lds\n"
    "v_mfma_f32_16x16x32_bf16 v[112:115], a[240:243], v[152:155], v[112:115]\n"
    "ds_read_b128 v[168:171], v235 offset:28672\n"
    "v_fma_f32 v199, v199, s51, s51\n"
    "v_mfma_f32_16x16x32_bf16 v[112:115], v[8:11], v[156:159], v[112:115]\n"
    "v_rcp_f32_e32 v198, v198\n"
    "v_mfma_f32_16x16x32_bf16 v[112:115], v[32:35], v[160:163], v[112:115]\n"
    "v_rcp_f32_e32 v199, v199\n"
    "v_mfma_f32_16x16x32_bf16 v[112:115], v[56:59], v[164:167], v[112:115]\n"
    "v_mul_f32_e32 v86, v86, v198\n"
    "v_mul_f32_e32 v87, v87, v199\n"
    "v_mfma_f32_16x16x32_bf16 v[116:119], a[172:175], v[172:175], v[116:119]\n"
    "ds_read_b128 v[172:175], v238 offset:28672\n"
    "v_cvt_pk_bf16_f32 v203, v86, v87\n"
    "v_mfma_f32_16x16x32_bf16 v[116:119], a[196:199], v[144:147], v[116:119]\n"
    "ds_read_b128 v[144:147], v235 offset:30720\n"
    "v_mfma_f32_16x16x32_bf16 v[116:119], a[220:223], v[148:151], v[116:119]\n"
    "ds_read_b128 v[148:151], v238 offset:30720\n"
    "v_mfma_f32_16x16x32_bf16 v[116:119], a[244:247], v[152:155], v[116:119]\n"
    "ds_read_b128 v[152:155], v235 offset:32768\n"
    "v_mfma_f32_16x16x32_bf16 v[116:119], v[12:15], v[156:159], v[116:119]\n"
    "ds_read_b128 v[156:159], v238 offset:32768\n"
    "v_mfma_f32_16x16x32_bf16 v[116:119], v[36:39], v[160:163], v[116:119]\n"
    "ds_read_b128 v[160:163], v235 offset:34816\n"
    "v_mfma_f32_16x16x32_bf16 v[116:119], v[60:63], v[164:167], v[116:119]\n"
    "ds_read_b128 v[164:167], v238 offset:34816\n"
    "s_waitcnt lgkmcnt(6)\n"
    "v_mfma_f32_16x16x32_bf16 v[112:115], a[8:11], v[168:171], v[112:115]\n"
    "ds_write_b128 v241, v[200:203] offset:0\n"
    "v_mfma_f32_16x16x32_bf16 v[112:115], a[32:35], v[172:175], v[112:115]\n"
    "v_fma_f32 v92, v92, v180, v188\n"
    "s_waitcnt lgkmcnt(5)\n"
    "v_mfma_f32_16x16x32_bf16 v[112:115], a[56:59], v[144:147], v[112:115]\n"
    "v_exp_f32_e32 v192, v88\n"
    "v_mfma_f32_16x16x32_bf16 v[112:115], a[80:83], v[148:151], v[112:115]\n"
    "v_fma_f32 v192, v192, s51, s51\n"
    "s_waitcnt lgkmcnt(3)\n"
    "v_mfma_f32_16x16x32_bf16 v[112:115], a[104:107], v[152:155], v[112:115]\n"
    "v_exp_f32_e32 v193, v89\n"
    "v_mfma_f32_16x16x32_bf16 v[112:115], a[128:131], v[156:159], v[112:115]\n"
    "v_fma_f32 v193, v193, s51, s51\n"
    "s_waitcnt lgkmcnt(1)\n"
    "v_mfma_f32_16x16x32_bf16 v[112:115], a[152:155], v[160:163], v[112:115]\n"
    "v_rcp_f32_e32 v192, v192\n"
    "v_mfma_f32_16x16x32_bf16 v[116:119], a[12:15], v[168:171], v[116:119]\n"
    "ds_read_b128 v[168:171], v235 offset:36864\n"
    "v_mul_f32_e32 v88, v88, v192\n"
    "v_mfma_f32_16x16x32_bf16 v[116:119], a[36:39], v[172:175], v[116:119]\n"
    "ds_read_b128 v[172:175], v238 offset:36864\n"
    "v_fma_f32 v93, v93, v181, v189\n"
    "v_mfma_f32_16x16x32_bf16 v[116:119], a[60:63], v[144:147], v[116:119]\n"
    "ds_read_b128 v[144:147], v235 offset:38912\n"
    "v_fma_f32 v94, v94, v182, v190\n"
    "v_mfma_f32_16x16x32_bf16 v[116:119], a[84:87], v[148:151], v[116:119]\n"
    "ds_read_b128 v[148:151], v238 offset:38912\n"
    "v_fma_f32 v95, v95, v183, v191\n"
    "v_mfma_f32_16x16x32_bf16 v[116:119], a[108:111], v[152:155], v[116:119]\n"
    "ds_read_b128 v[152:155], v235 offset:40960\n"
    "v_mfma_f32_16x16x32_bf16 v[116:119], a[132:135], v[156:159], v[116:119]\n"
    "ds_read_b128 v[156:159], v238 offset:40960\n"
    "v_mfma_f32_16x16x32_bf16 v[116:119], a[156:159], v[160:163], v[116:119]\n"
    "v_rcp_f32_e32 v193, v193\n"
    "v_mfma_f32_16x16x32_bf16 v[112:115], a[176:179], v[164:167], v[112:115]\n"
    "v_mul_f32_e32 v89, v89, v193\n"
    "s_waitcnt lgkmcnt(4)\n"
    "v_mfma_f32_16x16x32_bf16 v[112:115], a[200:203], v[168:171], v[112:115]\n"
    "v_cvt_pk_bf16_f32 v204, v88, v89\n"
    "v_mfma_f32_16x16x32_bf16 v[112:115], a[224:227], v[172:175], v[112:115]\n"
    "s_waitcnt vmcnt(14) lgkmcnt(0)\n"
    "s_barrier\n"
    "s_add_u32 s44, s48, 1389696\n"
    "s_add_u32 m0, s47, 28672\n"
    "s_nop 0\n"
    "buffer_load_dwordx4 v216, s[8:11], s44 offen offset:0 lds\n"
    "s_add_u32 m0, s47, 30592\n"
    "s_nop 0\n"
    "buffer_load_dwordx4 v216, s[8:11], s44 offen offset:128 lds\n"
    "s_add_u32 m0, s47, 32512\n"
    "s_nop 0\n"
    "buffer_load_dwordx4 v216, s[8:11], s44 offen offset:256 lds\n"
    "s_add_u32 m0, s47, 34432\n"
    "s_nop 0\n"
    "buffer_load_dwordx4 v216, s[8:11], s44 offen offset:384 lds\n"
    "s_add_u32 m0, s47, 36352\n"
    "s_nop 0\n"
    "buffer_load_dwordx4 v216, s[8:11], s44 offen offset:512 lds\n"
    "s_add_u32 m0, s47, 38272\n"
    "s_nop 0\n"
    "buffer_load_dwordx4 v216, s[8:11], s44 offen offset:640 lds\n"
    "s_add_u32 m0, s47, 40192\n"
    "s_nop 0\n"
    "buffer_load_dwordx4 v216, s[8:11], s44 offen offset:768 lds\n"
    "v_mfma_f32_16x16x32_bf16 v[112:115], a[248:251], v[144:147], v[112:115]\n"
    "ds_read_b128 v[160:163], v235 offset:57344\n"
    "v_mfma_f32_16x16x32_bf16 v[112:115], v[16:19], v[148:151], v[112:115]\n"
    "ds_read_b128 v[208:211], v242 offset:0\n"
    "s_add_u32 s50, s49, 13440\n"
    "v_mfma_f32_16x16x32_bf16 v[112:115], v[40:43], v[152:155], v[112:115]\n"
    "v_exp_f32_e32 v194, v90\n"
    "v_mfma_f32_16x16x32_bf16 v[112:115], v[64:67], v[156:159], v[112:115]\n"
    "v_exp_f32_e32 v195, v91\n"
    "v_mfma_f32_16x16x32_bf16 v[116:119], a[180:183], v[164:167], v[116:119]\n"
    "ds_read_b128 v[164:167], v238 offset:57344\n"
    "v_fma_f32 v194, v194, s51, s51\n"
    "v_mfma_f32_16x16x32_bf16 v[116:119], a[204:207], v[168:171], v[116:119]\n"
    "ds_read_b128 v[168:171], v235 offset:59392\n"
    "v_fma_f32 v195, v195, s51, s51\n"
    "v_mfma_f32_16x16x32_bf16 v[116:119], a[228:231], v[172:175], v[116:119]\n"
    "ds_read_b128 v[172:175], v238 offset:59392\n"
    "v_mfma_f32_16x16x32_bf16 v[116:119], a[252:255], v[144:147], v[116:119]\n"
    "ds_read_b128 v[144:147], v235 offset:61440\n"
    "v_mfma_f32_16x16x32_bf16 v[116:119], v[20:23], v[148:151], v[116:119]\n"
    "ds_read_b128 v[148:151], v238 offset:61440\n"
    "v_mfma_f32_16x16x32_bf16 v[116:119], v[44:47], v[152:155], v[116:119]\n"
    "ds_read_b128 v[152:155], v235 offset:63488\n"
    "v_mfma_f32_16x16x32_bf16 v[116:119], v[68:71], v[156:159], v[116:119]\n"
    "ds_read_b128 v[156:159], v238 offset:63488\n"
    "s_waitcnt lgkmcnt(6)\n"
    "v_mfma_f32_16x16x32_bf16 v[120:123], a[0:3], v[160:163], 0\n"
    "buffer_store_dwordx4 v[208:211], v243, s[16:19], s50 offen nt\n"
    "v_mfma_f32_16x16x32_bf16 v[120:123], a[24:27], v[164:167], v[120:123]\n"
    "s_waitcnt lgkmcnt(4)\n"
    "v_mfma_f32_16x16x32_bf16 v[120:123], a[48:51], v[168:171], v[120:123]\n"
    "v_rcp_f32_e32 v194, v194\n"
    "v_mfma_f32_16x16x32_bf16 v[120:123], a[72:75], v[172:175], v[120:123]\n"
    "v_mul_f32_e32 v90, v90, v194\n"
    "s_waitcnt lgkmcnt(2)\n"
    "v_mfma_f32_16x16x32_bf16 v[120:123], a[96:99], v[144:147], v[120:123]\n"
    "v_rcp_f32_e32 v195, v195\n"
    "v_mfma_f32_16x16x32_bf16 v[120:123], a[120:123], v[148:151], v[120:123]\n"
    "v_mul_f32_e32 v91, v91, v195\n"
    "s_waitcnt lgkmcnt(0)\n"
    "v_mfma_f32_16x16x32_bf16 v[120:123], a[144:147], v[152:155], v[120:123]\n"
    "v_cvt_pk_bf16_f32 v205, v90, v91\n"
    "v_fma_f32 v96, v96, v176, v184\n"
    "v_mfma_f32_16x16x32_bf16 v[124:127], a[4:7], v[160:163], 0\n"
    "ds_read_b128 v[160:163], v236 offset:0\n"
    "v_fma_f32 v97, v97, v177, v185\n"
    "v_mfma_f32_16x16x32_bf16 v[124:127], a[28:31], v[164:167], v[124:127]\n"
    "ds_read_b128 v[164:167], v239 offset:0\n"
    "v_fma_f32 v98, v98, v178, v186\n"
    "v_mfma_f32_16x16x32_bf16 v[124:127], a[52:55], v[168:171], v[124:127]\n"
    "ds_read_b128 v[168:171], v236 offset:2048\n"
    "v_fma_f32 v99, v99, v179, v187\n"
    "v_mfma_f32_16x16x32_bf16 v[124:127], a[76:79], v[172:175], v[124:127]\n"
    "ds_read_b128 v[172:175], v239 offset:2048\n"
    "v_mfma_f32_16x16x32_bf16 v[124:127], a[100:103], v[144:147], v[124:127]\n"
    "ds_read_b128 v[144:147], v236 offset:4096\n"
    "v_mfma_f32_16x16x32_bf16 v[124:127], a[124:127], v[148:151], v[124:127]\n"
    "ds_read_b128 v[148:151], v239 offset:4096\n"
    "v_mfma_f32_16x16x32_bf16 v[124:127], a[148:151], v[152:155], v[124:127]\n"
    "v_exp_f32_e32 v196, v92\n"
    "v_mfma_f32_16x16x32_bf16 v[120:123], a[168:171], v[156:159], v[120:123]\n"
    "v_fma_f32 v196, v196, s51, s51\n"
    "s_waitcnt lgkmcnt(4)\n"
    "v_mfma_f32_16x16x32_bf16 v[120:123], a[192:195], v[160:163], v[120:123]\n"
    "v_exp_f32_e32 v197, v93\n"
    "v_mfma_f32_16x16x32_bf16 v[120:123], a[216:219], v[164:167], v[120:123]\n"
    "s_waitcnt vmcnt(15) lgkmcnt(0)\n"
    "s_barrier\n"
    "s_add_u32 s44, s48, 1618176\n"
    "s_add_u32 m0, s47, 57344\n"
    "s_nop 0\n"
    "buffer_load_dwordx4 v216, s[8:11], s44 offen offset:0 lds\n"
    "s_add_u32 m0, s47, 59264\n"
    "s_nop 0\n"
    "buffer_load_dwordx4 v216, s[8:11], s44 offen offset:128 lds\n"
    "s_add_u32 m0, s47, 61184\n"
    "s_nop 0\n"
    "buffer_load_dwordx4 v216, s[8:11], s44 offen offset:256 lds\n"
    "s_add_u32 m0, s47, 63104\n"
    "s_nop 0\n"
    "buffer_load_dwordx4 v216, s[8:11], s44 offen offset:384 lds\n"
    "s_add_u32 m0, s47, 65024\n"
    "s_nop 0\n"
    "buffer_load_dwordx4 v216, s[8:11], s44 offen offset:512 lds\n"
    "s_add_u32 m0, s47, 66944\n"
    "s_nop 0\n"
    "buffer_load_dwordx4 v216, s[8:11], s44 offen offset:640 lds\n"
    "s_add_u32 m0, s47, 68864\n"
    "s_nop 0\n"
    "buffer_load_dwordx4 v216, s[8:11], s44 offen offset:768 lds\n"
    "v_mfma_f32_16x16x32_bf16 v[120:123], a[240:243], v[168:171], v[120:123]\n"
    "ds_read_b128 v[152:155], v236 offset:20480\n"
    "v_fma_f32 v197, v197, s51, s51\n"
    "v_mfma_f32_16x16x32_bf16 v[120:123], v[8:11], v[172:175], v[120:123]\n"
    "v_rcp_f32_e32 v196, v196\n"
    "v_mfma_f32_16x16x32_bf16 v[120:123], v[32:35], v[144:147], v[120:123]\n"
    "v_rcp_f32_e32 v197, v197\n"
    "v_mfma_f32_16x16x32_bf16 v[120:123], v[56:59], v[148:151], v[120:123]\n"
    "v_mul_f32_e32 v92, v92, v196\n"
    "v_mul_f32_e32 v93, v93, v197\n"
    "v_mfma_f32_16x16x32_bf16 v[124:127], a[172:175], v[156:159], v[124:127]\n"
    "ds_read_b128 v[156:159], v239 offset:20480\n"
    "v_cvt_pk_bf16_f32 v206, v92, v93\n"
    "v_mfma_f32_16x16x32_bf16 v[124:127], a[196:199], v[160:163], v[124:127]\n"
    "ds_read_b128 v[160:163], v236 offset:22528\n"
    "v_mfma_f32_16x16x32_bf16 v[124:127], a[220:223], v[164:167], v[124:127]\n"
    "ds_read_b128 v[164:167], v239 offset:22528\n"
    "v_mfma_f32_16x16x32_bf16 v[124:127], a[244:247], v[168:171], v[124:127]\n"
    "ds_read_b128 v[168:171], v236 offset:24576\n"
    "v_mfma_f32_16x16x32_bf16 v[124:127], v[12:15], v[172:175], v[124:127]\n"
    "ds_read_b128 v[172:175], v239 offset:24576\n"
    "v_mfma_f32_16x16x32_bf16 v[124:127], v[36:39], v[144:147], v[124:127]\n"
    "ds_read_b128 v[144:147], v236 offset:26624\n"
    "v_mfma_f32_16x16x32_bf16 v[124:127], v[60:63], v[148:151], v[124:127]\n"
    "ds_read_b128 v[148:151], v239 offset:26624\n"
    "s_waitcnt lgkmcnt(6)\n"
    "v_mfma_f32_16x16x32_bf16 v[120:123], a[8:11], v[152:155], v[120:123]\n"
    "v_exp_f32_e32 v198, v94\n"
    "v_mfma_f32_16x16x32_bf16 v[120:123], a[32:35], v[156:159], v[120:123]\n"
    "v_fma_f32 v198, v198, s51, s51\n"
    "s_waitcnt lgkmcnt(4)\n"
    "v_mfma_f32_16x16x32_bf16 v[120:123], a[56:59], v[160:163], v[120:123]\n"
    "v_exp_f32_e32 v199, v95\n"
    "v_mfma_f32_16x16x32_bf16 v[120:123], a[80:83], v[164:167], v[120:123]\n"
    "v_fma_f32 v199, v199, s51, s51\n"
    "s_waitcnt lgkmcnt(2)\n"
    "v_mfma_f32_16x16x32_bf16 v[120:123], a[104:107], v[168:171], v[120:123]\n"
    "v_rcp_f32_e32 v198, v198\n"
    "v_mfma_f32_16x16x32_bf16 v[120:123], a[128:131], v[172:175], v[120:123]\n"
    "v_mul_f32_e32 v94, v94, v198\n"
    "s_waitcnt lgkmcnt(0)\n"
    "v_mfma_f32_16x16x32_bf16 v[120:123], a[152:155], v[144:147], v[120:123]\n"
    "v_rcp_f32_e32 v199, v199\n"
    "v_mfma_f32_16x16x32_bf16 v[124:127], a[12:15], v[152:155], v[124:127]\n"
    "ds_read_b128 v[152:155], v236 offset:28672\n"
    "v_mul_f32_e32 v95, v95, v199\n"
    "v_mfma_f32_16x16x32_bf16 v[124:127], a[36:39], v[156:159], v[124:127]\n"
    "ds_read_b128 v[156:159], v239 offset:28672\n"
    "v_cvt_pk_bf16_f32 v207, v94, v95\n"
    "v_mfma_f32_16x16x32_bf16 v[124:127], a[60:63], v[160:163], v[124:127]\n"
    "ds_read_b128 v[160:163], v236 offset:30720\n"
    "v_mfma_f32_16x16x32_bf16 v[124:127], a[84:87], v[164:167], v[124:127]\n"
    "ds_read_b128 v[164:167], v239 offset:30720\n"
    "v_mfma_f32_16x16x32_bf16 v[124:127], a[108:111], v[168:171], v[124:127]\n"
    "ds_read_b128 v[168:171], v236 offset:32768\n"
    "v_mfma_f32_16x16x32_bf16 v[124:127], a[132:135], v[172:175], v[124:127]\n"
    "ds_read_b128 v[172:175], v239 offset:32768\n"
    "v_mfma_f32_16x16x32_bf16 v[124:127], a[156:159], v[144:147], v[124:127]\n"
    "v_exp_f32_e32 v192, v96\n"
    "v_mfma_f32_16x16x32_bf16 v[120:123], a[176:179], v[148:151], v[120:123]\n"
    "v_fma_f32 v192, v192, s51, s51\n"
    "s_waitcnt lgkmcnt(4)\n"
    "v_mfma_f32_16x16x32_bf16 v[120:123], a[200:203], v[152:155], v[120:123]\n"
    "v_exp_f32_e32 v193, v97\n"
    "v_mfma_f32_16x16x32_bf16 v[120:123], a[224:227], v[156:159], v[120:123]\n"
    "s_waitcnt vmcnt(15) lgkmcnt(0)\n"
    "s_barrier\n"
    "s_add_u32 s44, s48, 1619072\n"
    "s_add_u32 m0, s47, 86016\n"
    "s_nop 0\n"
    "buffer_load_dwordx4 v216, s[8:11], s44 offen offset:0 lds\n"
    "s_add_u32 m0, s47, 87936\n"
    "s_nop 0\n"
    "buffer_load_dwordx4 v216, s[8:11], s44 offen offset:128 lds\n"
    "s_add_u32 m0, s47, 89856\n"
    "s_nop 0\n"
    "buffer_load_dwordx4 v216, s[8:11], s44 offen offset:256 lds\n"
    "s_add_u32 m0, s47, 91776\n"
    "s_nop 0\n"
    "buffer_load_dwordx4 v216, s[8:11], s44 offen offset:384 lds\n"
    "s_add_u32 m0, s47, 93696\n"
    "s_nop 0\n"
    "buffer_load_dwordx4 v216, s[8:11], s44 offen offset:512 lds\n"
    "s_add_u32 m0, s47, 95616\n"
    "s_nop 0\n"
    "buffer_load_dwordx4 v216, s[8:11], s44 offen offset:640 lds\n"
    "s_add_u32 m0, s47, 97536\n"
    "s_nop 0\n"
    "buffer_load_dwordx4 v216, s[8:11], s44 offen offset:768 lds\n"
    "v_mfma_f32_16x16x32_bf16 v[120:123], a[248:251], v[160:163], v[120:123]\n"
    "ds_read_b128 v[144:147], v235 offset:0\n"
    "v_fma_f32 v193, v193, s51, s51\n"
    "v_mfma_f32_16x16x32_bf16 v[120:123], v[16:19], v[164:167], v[120:123]\n"
    "ds_write_b128 v241, v[204:207] offset:4096\n"
    "v_mfma_f32_16x16x32_bf16 v[120:123], v[40:43], v[168:171], v[120:123]\n"
    "v_rcp_f32_e32 v192, v192\n"
    "v_mfma_f32_16x16x32_bf16 v[120:123], v[64:67], v[172:175], v[120:123]\n"
    "v_rcp_f32_e32 v193, v193\n"
    "v_mfma_f32_16x16x32_bf16 v[124:127], a[180:183], v[148:151], v[124:127]\n"
    "ds_read_b128 v[148:151], v238 offset:0\n"
    "v_mul_f32_e32 v96, v96, v192\n"
    "v_mfma_f32_16x16x32_bf16 v[124:127], a[204:207], v[152:155], v[124:127]\n"
    "ds_read_b128 v[152:155], v235 offset:2048\n"
    "v_mul_f32_e32 v97, v97, v193\n"
    "v_mfma_f32_16x16x32_bf16 v[124:127], a[228:231], v[156:159], v[124:127]\n"
    "ds_read_b128 v[156:159], v238 offset:2048\n"
    "v_cvt_pk_bf16_f32 v200, v96, v97\n"
    "v_mfma_f32_16x16x32_bf16 v[124:127], a[252:255], v[160:163], v[124:127]\n"
    "ds_read_b128 v[160:163], v235 offset:4096\n"
    "v_fma_f32 v100, v100, v180, v188\n"
    "v_mfma_f32_16x16x32_bf16 v[124:127], v[20:23], v[164:167], v[124:127]\n"
    "ds_read_b128 v[164:167], v238 offset:4096\n"
    "v_fma_f32 v101, v101, v181, v189\n"
    "v_mfma_f32_16x16x32_bf16 v[124:127], v[44:47], v[168:171], v[124:127]\n"
    "ds_read_b128 v[168:171], v235 offset:6144\n"
    "v_fma_f32 v102, v102, v182, v190\n"
    "v_mfma_f32_16x16x32_bf16 v[124:127], v[68:71], v[172:175], v[124:127]\n"
    "ds_read_b128 v[172:175], v238 offset:6144\n"
    "s_waitcnt lgkmcnt(6)\n"
    "v_mfma_f32_16x16x32_bf16 v[128:131], a[0:3], v[144:147], 0\n"
    "v_exp_f32_e32 v194, v98\n"
    "v_mfma_f32_16x16x32_bf16 v[128:131], a[24:27], v[148:151], v[128:131]\n"
    "v_fma_f32 v194, v194, s51, s51\n"
    "s_waitcnt lgkmcnt(4)\n"
    "v_mfma_f32_16x16x32_bf16 v[128:131], a[48:51], v[152:155], v[128:131]\n"
    "v_exp_f32_e32 v195, v99\n"
    "v_mfma_f32_16x16x32_bf16 v[128:131], a[72:75], v[156:159], v[128:131]\n"
    "v_fma_f32 v195, v195, s51, s51\n"
    "s_waitcnt lgkmcnt(2)\n"
    "v_mfma_f32_16x16x32_bf16 v[128:131], a[96:99], v[160:163], v[128:131]\n"
    "v_rcp_f32_e32 v194, v194\n"
    "v_mfma_f32_16x16x32_bf16 v[128:131], a[120:123], v[164:167], v[128:131]\n"
    "v_mul_f32_e32 v98, v98, v194\n"
    "s_waitcnt lgkmcnt(0)\n"
    "v_mfma_f32_16x16x32_bf16 v[128:131], a[144:147], v[168:171], v[128:131]\n"
    "v_rcp_f32_e32 v195, v195\n"
    "v_mfma_f32_16x16x32_bf16 v[132:135], a[4:7], v[144:147], 0\n"
    "ds_read_b128 v[144:147], v235 offset:8192\n"
    "v_mul_f32_e32 v99, v99, v195\n"
    "v_mfma_f32_16x16x32_bf16 v[132:135], a[28:31], v[148:151], v[132:135]\n"
    "ds_read_b128 v[148:151], v238 offset:8192\n"
    "v_cvt_pk_bf16_f32 v201, v98, v99\n"
    "v_mfma_f32_16x16x32_bf16 v[132:135], a[52:55], v[152:155], v[132:135]\n"
    "ds_read_b128 v[152:155], v235 offset:10240\n"
    "v_fma_f32 v103, v103, v183, v191\n"
    "v_mfma_f32_16x16x32_bf16 v[132:135], a[76:79], v[156:159], v[132:135]\n"
    "ds_read_b128 v[156:159], v238 offset:10240\n"
    "v_fma_f32 v104, v104, v176, v184\n"
    "v_mfma_f32_16x16x32_bf16 v[132:135], a[100:103], v[160:163], v[132:135]\n"
    "ds_read_b128 v[160:163], v235 offset:12288\n"
    "v_fma_f32 v105, v105, v177, v185\n"
    "v_mfma_f32_16x16x32_bf16 v[132:135], a[124:127], v[164:167], v[132:135]\n"
    "ds_read_b128 v[164:167], v238 offset:12288\n"
    "v_fma_f32 v106, v106, v178, v186\n"
    "v_mfma_f32_16x16x32_bf16 v[132:135], a[148:151], v[168:171], v[132:135]\n"
    "v_exp_f32_e32 v196, v100\n"
    "v_mfma_f32_16x16x32_bf16 v[128:131], a[168:171], v[172:175], v[128:131]\n"
    "v_fma_f32 v196, v196, s51, s51\n"
    "s_waitcnt lgkmcnt(4)\n"
    "v_mfma_f32_16x16x32_bf16 v[128:131], a[192:195], v[144:147], v[128:131]\n"
    "v_exp_f32_e32 v197, v101\n"
    "v_mfma_f32_16x16x32_bf16 v[128:131], a[216:219], v[148:151], v[128:131]\n"
    "s_waitcnt vmcnt(15) lgkmcnt(0)\n"
    "s_barrier\n"
    "v_mfma_f32_16x16x32_bf16 v[128:131], a[240:243], v[152:155], v[128:131]\n"
    "ds_read_b128 v[168:171], v235 offset:28672\n"
    "v_fma_f32 v197, v197, s51, s51\n"
    "v_mfma_f32_16x16x32_bf16 v[128:131], v[8:11], v[156:159], v[128:131]\n"
    "ds_read_b128 v[212:215], v242 offset:4096\n"
    "s_add_u32 s50, s49, 242816\n"
    "v_mfma_f32_16x16x32_bf16 v[128:131], v[32:35], v[160:163], v[128:131]\n"
    "v_rcp_f32_e32 v196, v196\n"
    "v_mfma_f32_16x16x32_bf16 v[128:131], v[56:59], v[164:167], v[128:131]\n"
    "v_rcp_f32_e32 v197, v197\n"
    "v_mfma_f32_16x16x32_bf16 v[132:135], a[172:175], v[172:175], v[132:135]\n"
    "ds_read_b128 v[172:175], v238 offset:28672\n"
    "v_mul_f32_e32 v100, v100, v196\n"
    "v_mfma_f32_16x16x32_bf16 v[132:135], a[196:199], v[144:147], v[132:135]\n"
    "ds_read_b128 v[144:147], v235 offset:30720\n"
    "v_mul_f32_e32 v101, v101, v197\n"
    "v_mfma_f32_16x16x32_bf16 v[132:135], a[220:223], v[148:151], v[132:135]\n"
    "ds_read_b128 v[148:151], v238 offset:30720\n"
    "v_cvt_pk_bf16_f32 v202, v100, v101\n"
    "v_mfma_f32_16x16x32_bf16 v[132:135], a[244:247], v[152:155], v[132:135]\n"
    "ds_read_b128 v[152:155], v235 offset:32768\n"
    "v_fma_f32 v107, v107, v179, v187\n"
    "v_mfma_f32_16x16x32_bf16 v[132:135], v[12:15], v[156:159], v[132:135]\n"
    "ds_read_b128 v[156:159], v238 offset:32768\n"
    "v_mfma_f32_16x16x32_bf16 v[132:135], v[36:39], v[160:163], v[132:135]\n"
    "ds_read_b128 v[160:163], v235 offset:34816\n"
    "v_mfma_f32_16x16x32_bf16 v[132:135], v[60:63], v[164:167], v[132:135]\n"
    "ds_read_b128 v[164:167], v238 offset:34816\n"
    "s_waitcnt lgkmcnt(6)\n"
    "v_mfma_f32_16x16x32_bf16 v[128:131], a[8:11], v[168:171], v[128:131]\n"
    "buffer_store_dwordx4 v[212:215], v243, s[16:19], s50 offen nt\n"
    "v_mfma_f32_16x16x32_bf16 v[128:131], a[32:35], v[172:175], v[128:131]\n"
    "s_waitcnt lgkmcnt(4)\n"
    "v_mfma_f32_16x16x32_bf16 v[128:131], a[56:59], v[144:147], v[128:131]\n"
    "v_exp_f32_e32 v198, v102\n"
    "v_mfma_f32_16x16x32_bf16 v[128:131], a[80:83], v[148:151], v[128:131]\n"
    "v_fma_f32 v198, v198, s51, s51\n"
    "s_waitcnt lgkmcnt(2)\n"
    "v_mfma_f32_16x16x32_bf16 v[128:131], a[104:107], v[152:155], v[128:131]\n"
    "v_exp_f32_e32 v199, v103\n"
    "v_mfma_f32_16x16x32_bf16 v[128:131], a[128:131], v[156:159], v[128:131]\n"
    "v_fma_f32 v199, v199, s51, s51\n"
    "s_waitcnt lgkmcnt(0)\n"
    "v_mfma_f32_16x16x32_bf16 v[128:131], a[152:155], v[160:163], v[128:131]\n"
    "v_rcp_f32_e32 v198, v198\n"
    "v_mfma_f32_16x16x32_bf16 v[132:135], a[12:15], v[168:171], v[132:135]\n"
    "ds_read_b128 v[168:171], v235 offset:36864\n"
    "v_mul_f32_e32 v102, v102, v198\n"
    "v_mfma_f32_16x16x32_bf16 v[132:135], a[36:39], v[172:175], v[132:135]\n"
    "ds_read_b128 v[172:175], v238 offset:36864\n"
    "v_mfma_f32_16x16x32_bf16 v[132:135], a[60:63], v[144:147], v[132:135]\n"
    "ds_read_b128 v[144:147], v235 offset:38912\n"
    "v_mfma_f32_16x16x32_bf16 v[132:135], a[84:87], v[148:151], v[132:135]\n"
    "ds_read_b128 v[148:151], v238 offset:38912\n"
    "v_mfma_f32_16x16x32_bf16 v[132:135], a[108:111], v[152:155], v[132:135]\n"
    "ds_read_b128 v[152:155], v235 offset:40960\n"
    "v_mfma_f32_16x16x32_bf16 v[132:135], a[132:135], v[156:159], v[132:135]\n"
    "ds_read_b128 v[156:159], v238 offset:40960\n"
    "v_mfma_f32_16x16x32_bf16 v[132:135], a[156:159], v[160:163], v[132:135]\n"
    "v_rcp_f32_e32 v199, v199\n"
    "v_mfma_f32_16x16x32_bf16 v[128:131], a[176:179], v[164:167], v[128:131]\n"
    "v_mul_f32_e32 v103, v103, v199\n"
    "s_waitcnt lgkmcnt(4)\n"
    "v_mfma_f32_16x16x32_bf16 v[128:131], a[200:203], v[168:171], v[128:131]\n"
    "v_cvt_pk_bf16_f32 v203, v102, v103\n"
    "v_mfma_f32_16x16x32_bf16 v[128:131], a[224:227], v[172:175], v[128:131]\n"
    "s_waitcnt vmcnt(8) lgkmcnt(0)\n"
    "s_barrier\n"
    "v_mfma_f32_16x16x32_bf16 v[128:131], a[248:251], v[144:147], v[128:131]\n"
    "ds_read_b128 v[160:163], v235 offset:57344\n"
    "v_mfma_f32_16x16x32_bf16 v[128:131], v[16:19], v[148:151], v[128:131]\n"
    "ds_write_b128 v241, v[200:203] offset:8192\n"
    "v_mfma_f32_16x16x32_bf16 v[128:131], v[40:43], v[152:155], v[128:131]\n"
    "v_exp_f32_e32 v192, v104\n"
    "v_mfma_f32_16x16x32_bf16 v[128:131], v[64:67], v[156:159], v[128:131]\n"
    "v_exp_f32_e32 v193, v105\n"
    "v_mfma_f32_16x16x32_bf16 v[132:135], a[180:183], v[164:167], v[132:135]\n"
    "ds_read_b128 v[164:167], v238 offset:57344\n"
    "v_fma_f32 v192, v192, s51, s51\n"
    "v_mfma_f32_16x16x32_bf16 v[132:135], a[204:207], v[168:171], v[132:135]\n"
    "ds_read_b128 v[168:171], v235 offset:59392\n"
    "v_fma_f32 v193, v193, s51, s51\n"
    "v_mfma_f32_16x16x32_bf16 v[132:135], a[228:231], v[172:175], v[132:135]\n"
    "ds_read_b128 v[172:175], v238 offset:59392\n"
    "v_fma_f32 v108, v108, v180, v188\n"
    "v_mfma_f32_16x16x32_bf16 v[132:135], a[252:255], v[144:147], v[132:135]\n"
    "ds_read_b128 v[144:147], v235 offset:61440\n"
    "v_fma_f32 v109, v109, v181, v189\n"
    "v_mfma_f32_16x16x32_bf16 v[132:135], v[20:23], v[148:151], v[132:135]\n"
    "ds_read_b128 v[148:151], v238 offset:61440\n"
    "v_fma_f32 v110, v110, v182, v190\n"
    "v_mfma_f32_16x16x32_bf16 v[132:135], v[44:47], v[152:155], v[132:135]\n"
    "ds_read_b128 v[152:155], v235 offset:63488\n"
    "v_fma_f32 v111, v111, v183, v191\n"
    "v_mfma_f32_16x16x32_bf16 v[132:135], v[68:71], v[156:159], v[132:135]\n"
    "ds_read_b128 v[156:159], v238 offset:63488\n"
    "s_waitcnt lgkmcnt(6)\n"
    "v_mfma_f32_16x16x32_bf16 v[136:139], a[0:3], v[160:163], 0\n"
    "v_rcp_f32_e32 v192, v192\n"
    "v_mfma_f32_16x16x32_bf16 v[136:139], a[24:27], v[164:167], v[136:139]\n"
    "v_mul_f32_e32 v104, v104, v192\n"
    "s_waitcnt lgkmcnt(4)\n"
    "v_mfma_f32_16x16x32_bf16 v[136:139], a[48:51], v[168:171], v[136:139]\n"
    "v_rcp_f32_e32 v193, v193\n"
    "v_mfma_f32_16x16x32_bf16 v[136:139], a[72:75], v[172:175], v[136:139]\n"
    "v_mul_f32_e32 v105, v105, v193\n"
    "s_waitcnt lgkmcnt(2)\n"
    "v_mfma_f32_16x16x32_bf16 v[136:139], a[96:99], v[144:147], v[136:139]\n"
    "v_cvt_pk_bf16_f32 v204, v104, v105\n"
    "v_mfma_f32_16x16x32_bf16 v[136:139], a[120:123], v[148:151], v[136:139]\n"
    "s_waitcnt lgkmcnt(0)\n"
    "v_mfma_f32_16x16x32_bf16 v[136:139], a[144:147], v[152:155], v[136:139]\n"
    "v_exp_f32_e32 v194, v106\n"
    "v_mfma_f32_16x16x32_bf16 v[140:143], a[4:7], v[160:163], 0\n"
    "ds_read_b128 v[160:163], v236 offset:0\n"
    "v_fma_f32 v194, v194, s51, s51\n"
    "v_mfma_f32_16x16x32_bf16 v[140:143], a[28:31], v[164:167], v[140:143]\n"
    "ds_read_b128 v[164:167], v239 offset:0\n"
    "v_mfma_f32_16x16x32_bf16 v[140:143], a[52:55], v[168:171], v[140:143]\n"
    "ds_read_b128 v[168:171], v236 offset:2048\n"
    "v_mfma_f32_16x16x32_bf16 v[140:143], a[76:79], v[172:175], v[140:143]\n"
    "ds_read_b128 v[172:175], v239 offset:2048\n"
    "v_mfma_f32_16x16x32_bf16 v[140:143], a[100:103], v[144:147], v[140:143]\n"
    "ds_read_b128 v[144:147], v236 offset:4096\n"
    "v_mfma_f32_16x16x32_bf16 v[140:143], a[124:127], v[148:151], v[140:143]\n"
    "ds_read_b128 v[148:151], v239 offset:4096\n"
    "v_mfma_f32_16x16x32_bf16 v[140:143], a[148:151], v[152:155], v[140:143]\n"
    "v_exp_f32_e32 v195, v107\n"
    "v_mfma_f32_16x16x32_bf16 v[136:139], a[168:171], v[156:159], v[136:139]\n"
    "v_fma_f32 v195, v195, s51, s51\n"
    "s_waitcnt lgkmcnt(4)\n"
    "v_mfma_f32_16x16x32_bf16 v[136:139], a[192:195], v[160:163], v[136:139]\n"
    "v_rcp_f32_e32 v194, v194\n"
    "v_mfma_f32_16x16x32_bf16 v[136:139], a[216:219], v[164:167], v[136:139]\n"
    "s_waitcnt vmcnt(1) lgkmcnt(0)\n"
    "s_barrier\n"
    "v_mfma_f32_16x16x32_bf16 v[136:139], a[240:243], v[168:171], v[136:139]\n"
    "ds_read_b128 v[152:155], v236 offset:20480\n"
    "v_mul_f32_e32 v106, v106, v194\n"
    "v_mfma_f32_16x16x32_bf16 v[136:139], v[8:11], v[172:175], v[136:139]\n"
    "ds_read_b128 v[208:211], v242 offset:8192\n"
    "s_add_u32 s50, s49, 472192\n"
    "v_mfma_f32_16x16x32_bf16 v[136:139], v[32:35], v[144:147], v[136:139]\n"
    "v_rcp_f32_e32 v195, v195\n"
    "v_mfma_f32_16x16x32_bf16 v[136:139], v[56:59], v[148:151], v[136:139]\n"
    "v_mul_f32_e32 v107, v107, v195\n"
    "v_cvt_pk_bf16_f32 v205, v106, v107\n"
    "v_mfma_f32_16x16x32_bf16 v[140:143], a[172:175], v[156:159], v[140:143]\n"
    "ds_read_b128 v[156:159], v239 offset:20480\n"
    "v_fma_f32 v112, v112, v176, v184\n"
    "v_mfma_f32_16x16x32_bf16 v[140:143], a[196:199], v[160:163], v[140:143]\n"
    "ds_read_b128 v[160:163], v236 offset:22528\n"
    "v_fma_f32 v113, v113, v177, v185\n"
    "v_mfma_f32_16x16x32_bf16 v[140:143], a[220:223], v[164:167], v[140:143]\n"
    "ds_read_b128 v[164:167], v239 offset:22528\n"
    "v_fma_f32 v114, v114, v178, v186\n"
    "v_mfma_f32_16x16x32_bf16 v[140:143], a[244:247], v[168:171], v[140:143]\n"
    "ds_read_b128 v[168:171], v236 offset:24576\n"
    "v_fma_f32 v115, v115, v179, v187\n"
    "v_mfma_f32_16x16x32_bf16 v[140:143], v[12:15], v[172:175], v[140:143]\n"
    "ds_read_b128 v[172:175], v239 offset:24576\n"
    "v_mfma_f32_16x16x32_bf16 v[140:143], v[36:39], v[144:147], v[140:143]\n"
    "ds_read_b128 v[144:147], v236 offset:26624\n"
    "v_mfma_f32_16x16x32_bf16 v[140:143], v[60:63], v[148:151], v[140:143]\n"
    "ds_read_b128 v[148:151], v239 offset:26624\n"
    "s_waitcnt lgkmcnt(6)\n"
    "v_mfma_f32_16x16x32_bf16 v[136:139], a[8:11], v[152:155], v[136:139]\n"
    "buffer_store_dwordx4 v[208:211], v243, s[16:19], s50 offen nt\n"
    "v_mfma_f32_16x16x32_bf16 v[136:139], a[32:35], v[156:159], v[136:139]\n"
    "s_waitcnt lgkmcnt(4)\n"
    "v_mfma_f32_16x16x32_bf16 v[136:139], a[56:59], v[160:163], v[136:139]\n"
    "v_exp_f32_e32 v196, v108\n"
    "v_mfma_f32_16x16x32_bf16 v[136:139], a[80:83], v[164:167], v[136:139]\n"
    "v_fma_f32 v196, v196, s51, s51\n"
    "s_waitcnt lgkmcnt(2)\n"
    "v_mfma_f32_16x16x32_bf16 v[136:139], a[104:107], v[168:171], v[136:139]\n"
    "v_exp_f32_e32 v197, v109\n"
    "v_mfma_f32_16x16x32_bf16 v[136:139], a[128:131], v[172:175], v[136:139]\n"
    "v_fma_f32 v197, v197, s51, s51\n"
    "s_waitcnt lgkmcnt(0)\n"
    "v_mfma_f32_16x16x32_bf16 v[136:139], a[152:155], v[144:147], v[136:139]\n"
    "v_rcp_f32_e32 v196, v196\n"
    "v_mfma_f32_16x16x32_bf16 v[140:143], a[12:15], v[152:155], v[140:143]\n"
    "ds_read_b128 v[152:155], v236 offset:28672\n"
    "v_mul_f32_e32 v108, v108, v196\n"
    "v_mfma_f32_16x16x32_bf16 v[140:143], a[36:39], v[156:159], v[140:143]\n"
    "ds_read_b128 v[156:159], v239 offset:28672\n"
    "v_mfma_f32_16x16x32_bf16 v[140:143], a[60:63], v[160:163], v[140:143]\n"
    "ds_read_b128 v[160:163], v236 offset:30720\n"
    "v_mfma_f32_16x16x32_bf16 v[140:143], a[84:87], v[164:167], v[140:143]\n"
    "ds_read_b128 v[164:167], v239 offset:30720\n"
    "v_mfma_f32_16x16x32_bf16 v[140:143], a[108:111], v[168:171], v[140:143]\n"
    "ds_read_b128 v[168:171], v236 offset:32768\n"
    "v_mfma_f32_16x16x32_bf16 v[140:143], a[132:135], v[172:175], v[140:143]\n"
    "ds_read_b128 v[172:175], v239 offset:32768\n"
    "v_mfma_f32_16x16x32_bf16 v[140:143], a[156:159], v[144:147], v[140:143]\n"
    "v_rcp_f32_e32 v197, v197\n"
    "v_mfma_f32_16x16x32_bf16 v[136:139], a[176:179], v[148:151], v[136:139]\n"
    "v_mul_f32_e32 v109, v109, v197\n"
    "s_waitcnt lgkmcnt(4)\n"
    "v_mfma_f32_16x16x32_bf16 v[136:139], a[200:203], v[152:155], v[136:139]\n"
    "v_cvt_pk_bf16_f32 v206, v108, v109\n"
    "v_mfma_f32_16x16x32_bf16 v[136:139], a[224:227], v[156:159], v[136:139]\n"
    "s_waitcnt lgkmcnt(2)\n"
    "v_mfma_f32_16x16x32_bf16 v[136:139], a[248:251], v[160:163], v[136:139]\n"
    "v_exp_f32_e32 v198, v110\n"
    "v_mfma_f32_16x16x32_bf16 v[136:139], v[16:19], v[164:167], v[136:139]\n"
    "v_fma_f32 v198, v198, s51, s51\n"
    "s_waitcnt lgkmcnt(0)\n"
    "v_mfma_f32_16x16x32_bf16 v[136:139], v[40:43], v[168:171], v[136:139]\n"
    "v_exp_f32_e32 v199, v111\n"
    "v_mfma_f32_16x16x32_bf16 v[136:139], v[64:67], v[172:175], v[136:139]\n"
    "v_fma_f32 v199, v199, s51, s51\n"
    "v_mfma_f32_16x16x32_bf16 v[140:143], a[180:183], v[148:151], v[140:143]\n"
    "v_rcp_f32_e32 v198, v198\n"
    "v_mfma_f32_16x16x32_bf16 v[140:143], a[204:207], v[152:155], v[140:143]\n"
    "v_rcp_f32_e32 v199, v199\n"
    "v_mfma_f32_16x16x32_bf16 v[140:143], a[228:231], v[156:159], v[140:143]\n"
    "v_mul_f32_e32 v110, v110, v198\n"
    "v_mul_f32_e32 v111, v111, v199\n"
    "v_mfma_f32_16x16x32_bf16 v[140:143], a[252:255], v[160:163], v[140:143]\n"
    "v_cvt_pk_bf16_f32 v207, v110, v111\n"
    "v_mfma_f32_16x16x32_bf16 v[140:143], v[20:23], v[164:167], v[140:143]\n"
    "ds_write_b128 v241, v[204:207] offset:12288\n"
    "v_mfma_f32_16x16x32_bf16 v[140:143], v[44:47], v[168:171], v[140:143]\n"
    "v_exp_f32_e32 v192, v112\n"
    "v_mfma_f32_16x16x32_bf16 v[140:143], v[68:71], v[172:175], v[140:143]\n"
    "s_nop 7\n"
    "v_exp_f32_e32 v193, v113\n"
    "v_fma_f32 v192, v192, s51, s51\n"
    "v_rcp_f32_e32 v192, v192\n"
    "v_fma_f32 v193, v193, s51, s51\n"
    "v_rcp_f32_e32 v193, v193\n"
    "v_mul_f32_e32 v112, v112, v192\n"
    "v_exp_f32_e32 v194, v114\n"
    "v_mul_f32_e32 v113, v113, v193\n"
    "v_exp_f32_e32 v195, v115\n"
    "v_cvt_pk_bf16_f32 v200, v112, v113\n"
    "v_fma_f32 v194, v194, s51, s51\n"
    "v_fma_f32 v195, v195, s51, s51\n"
    "v_rcp_f32_e32 v194, v194\n"
    "v_fma_f32 v116, v116, v180, v188\n"
    "v_rcp_f32_e32 v195, v195\n"
    "v_mul_f32_e32 v114, v114, v194\n"
    "v_exp_f32_e32 v196, v116\n"
    "v_mul_f32_e32 v115, v115, v195\n"
    "v_cvt_pk_bf16_f32 v201, v114, v115\n"
    "v_fma_f32 v117, v117, v181, v189\n"
    "v_exp_f32_e32 v197, v117\n"
    "v_fma_f32 v196, v196, s51, s51\n"
    "v_rcp_f32_e32 v196, v196\n"
    "v_fma_f32 v197, v197, s51, s51\n"
    "v_rcp_f32_e32 v197, v197\n"
    "v_mul_f32_e32 v116, v116, v196\n"
    "v_mul_f32_e32 v117, v117, v197\n"
    "v_cvt_pk_bf16_f32 v202, v116, v117\n"
    "v_fma_f32 v118, v118, v182, v190\n"
    "v_fma_f32 v119, v119, v183, v191\n"
    "v_exp_f32_e32 v198, v118\n"
    "v_fma_f32 v120, v120, v176, v184\n"
    "v_exp_f32_e32 v199, v119\n"
    "v_fma_f32 v198, v198, s51, s51\n"
    "v_rcp_f32_e32 v198, v198\n"
    "v_fma_f32 v199, v199, s51, s51\n"
    "v_rcp_f32_e32 v199, v199\n"
    "v_mul_f32_e32 v118, v118, v198\n"
    "v_exp_f32_e32 v192, v120\n"
    "v_mul_f32_e32 v119, v119, v199\n"
    "v_cvt_pk_bf16_f32 v203, v118, v119\n"
    "v_fma_f32 v121, v121, v177, v185\n"
    "ds_write_b128 v241, v[200:203] offset:0\n"
    "s_add_u32 s43, s49, 1160320\n"
    "v_exp_f32_e32 v193, v121\n"
    "v_fma_f32 v192, v192, s51, s51\n"
    "v_rcp_f32_e32 v192, v192\n"
    "v_fma_f32 v193, v193, s51, s51\n"
    "v_rcp_f32_e32 v193, v193\n"
    "v_mul_f32_e32 v120, v120, v192\n"
    "v_mul_f32_e32 v121, v121, v193\n"
    "v_cvt_pk_bf16_f32 v204, v120, v121\n"
    "v_fma_f32 v122, v122, v178, v186\n"
    "v_fma_f32 v123, v123, v179, v187\n"
    "v_exp_f32_e32 v194, v122\n"
    "v_fma_f32 v124, v124, v180, v188\n"
    "v_exp_f32_e32 v195, v123\n"
    "v_fma_f32 v194, v194, s51, s51\n"
    "v_rcp_f32_e32 v194, v194\n"
    "v_fma_f32 v195, v195, s51, s51\n"
    "v_rcp_f32_e32 v195, v195\n"
    "v_mul_f32_e32 v122, v122, v194\n"
    "v_exp_f32_e32 v196, v124\n"
    "v_mul_f32_e32 v123, v123, v195\n"
    "v_cvt_pk_bf16_f32 v205, v122, v123\n"
    "v_fma_f32 v125, v125, v181, v189\n"
    "v_exp_f32_e32 v197, v125\n"
    "v_fma_f32 v196, v196, s51, s51\n"
    "v_rcp_f32_e32 v196, v196\n"
    "v_fma_f32 v197, v197, s51, s51\n"
    "v_rcp_f32_e32 v197, v197\n"
    "v_mul_f32_e32 v124, v124, v196\n"
    "v_mul_f32_e32 v125, v125, v197\n"
    "v_cvt_pk_bf16_f32 v206, v124, v125\n"
    "v_fma_f32 v126, v126, v182, v190\n"
    "v_fma_f32 v127, v127, v183, v191\n"
    "v_exp_f32_e32 v198, v126\n"
    "v_fma_f32 v128, v128, v176, v184\n"
    "v_exp_f32_e32 v199, v127\n"
    "v_fma_f32 v198, v198, s51, s51\n"
    "v_rcp_f32_e32 v198, v198\n"
    "v_fma_f32 v199, v199, s51, s51\n"
    "v_rcp_f32_e32 v199, v199\n"
    "v_mul_f32_e32 v126, v126, v198\n"
    "v_exp_f32_e32 v192, v128\n"
    "v_mul_f32_e32 v127, v127, v199\n"
    "v_cvt_pk_bf16_f32 v207, v126, v127\n"
    "v_fma_f32 v129, v129, v177, v185\n"
    "buffer_store_dwordx4 v[204:207], v252, s[16:19], s43 offen nt\n"
    "s_add_u32 s43, s49, 1389696\n"
    "v_exp_f32_e32 v193, v129\n"
    "v_fma_f32 v192, v192, s51, s51\n"
    "v_rcp_f32_e32 v192, v192\n"
    "v_fma_f32 v193, v193, s51, s51\n"
    "v_rcp_f32_e32 v193, v193\n"
    "v_mul_f32_e32 v128, v128, v192\n"
    "v_mul_f32_e32 v129, v129, v193\n"
    "v_cvt_pk_bf16_f32 v200, v128, v129\n"
    "v_fma_f32 v130, v130, v178, v186\n"
    "v_fma_f32 v131, v131, v179, v187\n"
    "v_exp_f32_e32 v194, v130\n"
    "v_fma_f32 v132, v132, v180, v188\n"
    "v_exp_f32_e32 v195, v131\n"
    "v_fma_f32 v194, v194, s51, s51\n"
    "v_rcp_f32_e32 v194, v194\n"
    "v_fma_f32 v195, v195, s51, s51\n"
    "v_rcp_f32_e32 v195, v195\n"
    "v_mul_f32_e32 v130, v130, v194\n"
    "v_exp_f32_e32 v196, v132\n"
    "v_mul_f32_e32 v131, v131, v195\n"
    "v_cvt_pk_bf16_f32 v201, v130, v131\n"
    "v_fma_f32 v133, v133, v181, v189\n"
    "v_exp_f32_e32 v197, v133\n"
    "v_fma_f32 v196, v196, s51, s51\n"
    "v_rcp_f32_e32 v196, v196\n"
    "v_fma_f32 v197, v197, s51, s51\n"
    "v_rcp_f32_e32 v197, v197\n"
    "v_mul_f32_e32 v132, v132, v196\n"
    "v_mul_f32_e32 v133, v133, v197\n"
    "v_cvt_pk_bf16_f32 v202, v132, v133\n"
    "v_fma_f32 v134, v134, v182, v190\n"
    "v_fma_f32 v135, v135, v183, v191\n"
    "v_exp_f32_e32 v198, v134\n"
    "v_fma_f32 v136, v136, v176, v184\n"
    "v_exp_f32_e32 v199, v135\n"
    "v_fma_f32 v198, v198, s51, s51\n"
    "v_rcp_f32_e32 v198, v198\n"
    "v_fma_f32 v199, v199, s51, s51\n"
    "v_rcp_f32_e32 v199, v199\n"
    "v_mul_f32_e32 v134, v134, v198\n"
    "v_exp_f32_e32 v192, v136\n"
    "v_mul_f32_e32 v135, v135, v199\n"
    "v_cvt_pk_bf16_f32 v203, v134, v135\n"
    "v_fma_f32 v137, v137, v177, v185\n"
    "buffer_store_dwordx4 v[200:203], v252, s[16:19], s43 offen nt\n"
    "s_add_u32 s43, s49, 1619072\n"
    "v_exp_f32_e32 v193, v137\n"
    "v_fma_f32 v192, v192, s51, s51\n"
    "v_rcp_f32_e32 v192, v192\n"
    "v_fma_f32 v193, v193, s51, s51\n"
    "v_rcp_f32_e32 v193, v193\n"
    "v_mul_f32_e32 v136, v136, v192\n"
    "v_mul_f32_e32 v137, v137, v193\n"
    "v_cvt_pk_bf16_f32 v204, v136, v137\n"
    "v_fma_f32 v138, v138, v178, v186\n"
    "v_fma_f32 v139, v139, v179, v187\n"
    "v_exp_f32_e32 v194, v138\n"
    "v_fma_f32 v140, v140, v180, v188\n"
    "v_exp_f32_e32 v195, v139\n"
    "v_fma_f32 v194, v194, s51, s51\n"
    "v_rcp_f32_e32 v194, v194\n"
    "v_fma_f32 v195, v195, s51, s51\n"
    "v_rcp_f32_e32 v195, v195\n"
    "v_mul_f32_e32 v138, v138, v194\n"
    "v_exp_f32_e32 v196, v140\n"
    "v_mul_f32_e32 v139, v139, v195\n"
    "v_cvt_pk_bf16_f32 v205, v138, v139\n"
    "v_fma_f32 v141, v141, v181, v189\n"
    "v_exp_f32_e32 v197, v141\n"
    "v_fma_f32 v196, v196, s51, s51\n"
    "v_rcp_f32_e32 v196, v196\n"
    "v_fma_f32 v197, v197, s51, s51\n"
    "v_rcp_f32_e32 v197, v197\n"
    "v_mul_f32_e32 v140, v140, v196\n"
    "v_mul_f32_e32 v141, v141, v197\n"
    "v_cvt_pk_bf16_f32 v206, v140, v141\n"
    "v_fma_f32 v142, v142, v182, v190\n"
    "v_fma_f32 v143, v143, v183, v191\n"
    "v_exp_f32_e32 v198, v142\n"
    "v_exp_f32_e32 v199, v143\n"
    "v_fma_f32 v198, v198, s51, s51\n"
    "v_rcp_f32_e32 v198, v198\n"
    "v_fma_f32 v199, v199, s51, s51\n"
    "v_rcp_f32_e32 v199, v199\n"
    "v_mul_f32_e32 v142, v142, v198\n"
    "v_mul_f32_e32 v143, v143, v199\n"
    "v_cvt_pk_bf16_f32 v207, v142, v143\n"
    "buffer_store_dwordx4 v[204:207], v252, s[16:19], s43 offen nt\n"
    "s_waitcnt lgkmcnt(0)\n"
    "s_barrier\n"
    "ds_read_b128 v[212:215], v242 offset:12288\n"
    "s_add_u32 s50, s49, 701568\n"
    "s_waitcnt lgkmcnt(0)\n"
    "buffer_store_dwordx4 v[212:215], v243, s[16:19], s50 offen nt\n"
    "ds_read_b128 v[208:211], v242 offset:0\n"
    "s_add_u32 s50, s49, 930944\n"
    "s_waitcnt lgkmcnt(0)\n"
    "buffer_store_dwordx4 v[208:211], v243, s[16:19], s50 offen nt\n"
    "6:\n"
    "9:\n"
    :
    : [blk] "s"(blk), [wave] "s"(wave), [kp] "s"(kp)
    : "memory", "vcc", "scc", "m0", "s8","s9","s10","s11","s12","s13","s14","s15","s16","s17","s18","s19","s20","s21","s22","s23","s24","s25","s26","s27","s28","s29","s30","s31","s34","s36","s37","s38","s39","s40","s41","s42","s43","s44","s45","s46","s47","s48","s49","s50","s51","s52","s53","s54","s55","s56","s57","s58","s59","s60","s61","s62","s63","s64","s65","s66","s67","s68","s69","s70","s71","s72","s73","s74","s75","s76","s77","s78","s79","s80","s81",
      "v0","v1","v2","v3","v4","v5","v6","v7","v8","v9","v10","v11","v12","v13","v14","v15","v16","v17","v18","v19","v20","v21","v22","v23","v24","v25","v26","v27","v28","v29","v30","v31","v32","v33","v34","v35","v36","v37","v38","v39","v40","v41","v42","v43","v44","v45","v46","v47","v48","v49","v50","v51","v52","v53","v54","v55","v56","v57","v58","v59","v60","v61","v62","v63","v64","v65","v66","v67","v68","v69","v70","v71","v72","v73","v74","v75","v76","v77","v78","v79","v80","v81","v82","v83","v84","v85","v86","v87","v88","v89","v90","v91","v92","v93","v94","v95","v96","v97","v98","v99","v100","v101","v102","v103","v104","v105","v106","v107","v108","v109","v110","v111","v112","v113","v114","v115","v116","v117","v118","v119","v120","v121","v122","v123","v124","v125","v126","v127","v128","v129","v130","v131","v132","v133","v134","v135","v136","v137","v138","v139","v140","v141","v142","v143","v144","v145","v146","v147","v148","v149","v150","v151","v152","v153","v154","v155","v156","v157","v158","v159","v160","v161","v162","v163","v164","v165","v166","v167","v168","v169","v170","v171","v172","v173","v174","v175","v176","v177","v178","v179","v180","v181","v182","v183","v184","v185","v186","v187","v188","v189","v190","v191","v192","v193","v194","v195","v196","v197","v198","v199","v200","v201","v202","v203","v204","v205","v206","v207","v208","v209","v210","v211","v212","v213","v214","v215","v216","v217","v218","v219","v220","v221","v222","v223","v224","v225","v226","v227","v228","v229","v230","v231","v232","v233","v234","v235","v236","v237","v238","v239","v240","v241","v242","v243","v244","v245","v246","v247","v248","v249","v250","v251","v252","v253","v254","v255",
      "a0","a1","a2","a3","a4","a5","a6","a7","a8","a9","a10","a11","a12","a13","a14","a15","a16","a17","a18","a19","a20","a21","a22","a23","a24","a25","a26","a27","a28","a29","a30","a31","a32","a33","a34","a35","a36","a37","a38","a39","a40","a41","a42","a43","a44","a45","a46","a47","a48","a49","a50","a51","a52","a53","a54","a55","a56","a57","a58","a59","a60","a61","a62","a63","a64","a65","a66","a67","a68","a69","a70","a71","a72","a73","a74","a75","a76","a77","a78","a79","a80","a81","a82","a83","a84","a85","a86","a87","a88","a89","a90","a91","a92","a93","a94","a95","a96","a97","a98","a99","a100","a101","a102","a103","a104","a105","a106","a107","a108","a109","a110","a111","a112","a113","a114","a115","a116","a117","a118","a119","a120","a121","a122","a123","a124","a125","a126","a127","a128","a129","a130","a131","a132","a133","a134","a135","a136","a137","a138","a139","a140","a141","a142","a143","a144","a145","a146","a147","a148","a149","a150","a151","a152","a153","a154","a155","a156","a157","a158","a159","a160","a161","a162","a163","a164","a165","a166","a167","a168","a169","a170","a171","a172","a173","a174","a175","a176","a177","a178","a179","a180","a181","a182","a183","a184","a185","a186","a187","a188","a189","a190","a191","a192","a193","a194","a195","a196","a197","a198","a199","a200","a201","a202","a203","a204","a205","a206","a207","a208","a209","a210","a211","a212","a213","a214","a215","a216","a217","a218","a219","a220","a221","a222","a223","a224","a225","a226","a227","a228","a229","a230","a231","a232","a233","a234","a235","a236","a237","a238","a239","a240","a241","a242","a243","a244","a245","a246","a247","a248","a249","a250","a251","a252","a253","a254","a255");
}

extern "C" void kernel_launch(void* const* d_in, const int* in_sizes, int n_in, void* d_out, int out_size, void* d_ws, size_t ws_size,
                              hipStream_t stream) {
  (void)in_sizes; (void)n_in; (void)out_size; (void)d_ws; (void)ws_size;
  (void)hipFuncSetAttribute((const void*)conv_bn_silu_kernel, hipFuncAttributeMaxDynamicSharedMemorySize, 163840);
  conv_bn_silu_kernel<<<256, 256, 163840, stream>>>(d_in[0], d_in[1], d_in[2], d_in[3], d_in[4], d_in[5], d_out);
}
```
